```python
import torch

def _softmax(x, axis=-1):
    return torch.softmax(x, dim=axis)
def _gelu(x):
    return torch.nn.functional.gelu(x, approximate="tanh")
def _silu(x):
    return torch.nn.functional.silu(x)
def _sigmoid(x):
    return torch.sigmoid(x)
def _layernorm(x, axis=-1, eps=1e-5):
    m = x.mean(dim=axis, keepdim=True)
    v = x.var(dim=axis, keepdim=True, unbiased=False)
    return (x - m) / torch.sqrt(v + eps)
def _scatter_add(tgt, idx, src, axis=0):
    return tgt.clone().index_add_(axis, idx, src)
def _segment_sum(src, seg, n):
    out = torch.zeros((n,) + src.shape[1:], dtype=src.dtype)
    return out.index_add_(0, seg, src)
def _pad(x, pw):
    flat = tuple(v for pair in reversed(pw) for v in pair)
    return torch.nn.functional.pad(x, flat)

kernel_name = 'sequential_preLN__gqa_block_cached__swiglu_ffn__decode'

def setup_inputs():

    _w = (lambda a: (lambda t: t.to(torch.bfloat16) if t.is_floating_point() else t)(torch.as_tensor(a)))
    def _A_setup():
        B, Sq, D = 2, 1, 512
        Hq, Hkv, Sk, Dh = 32, 8, 32768, 128
        assert Hq % Hkv == 0, (Hq, Hkv)
        return {
            "x": _w((lambda *s: torch.randn(*s, dtype=torch.bfloat16) * 0.05)(B, Sq, D)),
            "k_cache": _w((lambda *s: torch.randn(*s, dtype=torch.bfloat16) * 3.2)(B, Hkv, Sk, Dh)),
            "v_cache": _w((lambda *s: torch.randn(*s, dtype=torch.bfloat16) * 0.4)(B, Hkv, Sk, Dh)),
            "wq": _w((lambda *s: torch.randn(*s, dtype=torch.bfloat16) * 3.2)(D, Hq * Dh) * (D ** -0.5)),
            "wo": _w((lambda *s: torch.randn(*s, dtype=torch.bfloat16) * 0.4)(Hq * Dh, D) * ((Hq * Dh) ** -0.5)),
        }
    _A_in = _A_setup()

    def _B_setup():
        B, S, D, F = 2, 1, 512, 1792
        return {
            "x": _w((lambda *s: torch.randn(*s, dtype=torch.bfloat16) * 0.05)(B, S, D)),
            "w_gate": _w((lambda *s: torch.randn(*s, dtype=torch.bfloat16) * 0.05)(D, F)),
            "w_up": _w((lambda *s: torch.randn(*s, dtype=torch.bfloat16) * 0.05)(D, F)),
            "w_down": _w((lambda *s: torch.randn(*s, dtype=torch.bfloat16) * 0.05)(F, D)),
        }
    _B_in = _B_setup()

    return {
        "A_x": _A_in["x"],
        "A_k_cache": _A_in["k_cache"],
        "A_v_cache": _A_in["v_cache"],
        "A_wq": _A_in["wq"],
        "A_wo": _A_in["wo"],
        "B_x": _B_in["x"],
        "B_w_gate": _B_in["w_gate"],
        "B_w_up": _B_in["w_up"],
        "B_w_down": _B_in["w_down"],
    }

def reference(A_x, A_k_cache, A_v_cache, A_wq, A_wo, B_x, B_w_gate, B_w_up, B_w_down):
    def _A_ref(x, k_cache, v_cache, wq, wo):
        B, Sq, D = x.shape
        Hq, Dh = 32, 128
        Hkv = k_cache.shape[1]
        rep = Hq // Hkv
        q = (lambda x, *d: x.permute(*d))((x @ wq).reshape(B, Sq, Hq, Dh), 0, 2, 1, 3)
        k = (lambda x, n, axis: torch.repeat_interleave(x, n, dim=axis))(k_cache, rep, axis=1)
        v = (lambda x, n, axis: torch.repeat_interleave(x, n, dim=axis))(v_cache, rep, axis=1)
        scores = torch.einsum("bhqd,bhkd->bhqk", q, k) * (Dh ** -0.5)
        p = _softmax(scores, axis=-1)
        o = torch.einsum("bhqk,bhkd->bhqd", p, v)
        return (lambda x, *d: x.permute(*d))(o, 0, 2, 1, 3).reshape(B, Sq, Hq * Dh) @ wo

    def _B_ref(x, w_gate, w_up, w_down):
        g = _silu(x @ w_gate)
        u = x @ w_up
        return (g * u) @ w_down

    x = A_x
    h = x + _A_ref(_layernorm(x, axis=-1), A_k_cache, A_v_cache, A_wq, A_wo)
    h2 = h + _B_ref(_layernorm(h, axis=-1), B_w_gate, B_w_up, B_w_down)
    return h2
```

```cpp
#include <hip/hip_runtime.h>
#include <hip/hip_bf16.h>
#include <stdint.h>

#define MIMIC 1

typedef unsigned short u16;
typedef unsigned u32x4 __attribute__((ext_vector_type(4)));
typedef unsigned u32x2 __attribute__((ext_vector_type(2)));
typedef float f32x4 __attribute__((ext_vector_type(4)));
typedef short bf16x8 __attribute__((ext_vector_type(8)));
typedef __bf16 bf16x8_t __attribute__((ext_vector_type(8)));
typedef short bf16x4 __attribute__((ext_vector_type(4)));

constexpr int Bn = 2, D = 512, HQ = 32, HKV = 8, SK = 32768, DH = 128, FF = 1792;
constexpr int NSPLIT = 16, KEYS_PER_WG = SK / NSPLIT, NW = 8, KEYS_PER_WAVE = KEYS_PER_WG / NW, NT = KEYS_PER_WAVE / 16;

__device__ __forceinline__ float bf2f(u16 b) { return __builtin_bit_cast(float, (unsigned)b << 16); }
__device__ __forceinline__ u16 f2bf(float f) { __bf16 h = (__bf16)f; return __builtin_bit_cast(u16, h); }
__device__ __forceinline__ float rbf(float f) {
  return bf2f(f2bf(f));
}

__device__ __forceinline__ float wave_sum(float v) {
  for (int o = 32; o > 0; o >>= 1) v += __shfl_xor(v, o);
  return v;
}

__device__ __forceinline__ void ln_row(const float* x8, float* out8) {
  float s = 0.f;
  for (int i = 0; i < 8; ++i) s += x8[i];
  s = wave_sum(s);
  float mean = s * (1.0f / D);
  float v = 0.f;
  for (int i = 0; i < 8; ++i) { float d = x8[i] - mean; v += d * d; }
  v = wave_sum(v) * (1.0f / D);
  float mb = rbf(mean), vb = rbf(v);
  float den = rbf(sqrtf(rbf(vb + 1e-5f)));
  for (int i = 0; i < 8; ++i) out8[i] = rbf(rbf(x8[i] - mb) / den);
}

__global__ void __launch_bounds__(256) k_q(const u16* x, const u16* wq, u16* q) {
  __shared__ float xn[2][D];
  __shared__ float red[4][2][64];
  int tid = threadIdx.x, lane = tid & 63, w = tid >> 6;
  if (w < 2) {
    float a[8], o[8];
    for (int i = 0; i < 8; ++i) a[i] = bf2f(x[w * D + lane * 8 + i]);
    ln_row(a, o);
    for (int i = 0; i < 8; ++i) xn[w][lane * 8 + i] = o[i];
  }
  __syncthreads();
  int col = blockIdx.x * 64 + lane;
  float a0 = 0.f, a1 = 0.f;
  for (int k = w * 128; k < w * 128 + 128; ++k) {
    float wv = bf2f(wq[(size_t)k * 4096 + col]);
    a0 += xn[0][k] * wv; a1 += xn[1][k] * wv;
  }
  red[w][0][lane] = a0; red[w][1][lane] = a1;
  __syncthreads();
  if (w < 2) {
    float s = red[0][w][lane] + red[1][w][lane] + red[2][w][lane] + red[3][w][lane];
    q[w * 4096 + col] = f2bf(s);
  }
}

__global__ void __launch_bounds__(512, 2) k_attn(const u16* __restrict__ q, const u16* __restrict__ kc, const u16* __restrict__ vc, float* __restrict__ po, float* __restrict__ pml) {
  __shared__ float sm[NW][4];
  __shared__ float sl[NW][4];
  __shared__ __attribute__((aligned(16))) float so[NW][512];
  const int g = blockIdx.x, kvh = g & 7, rest = g >> 3, b = rest >> 4, split = rest & 15;
  const int tid = threadIdx.x, lane = tid & 63, w = __builtin_amdgcn_readfirstlane(tid >> 6);
  const int r = lane >> 4, c = lane & 15;
  const size_t base = ((size_t)(b * HKV + kvh) * SK + (size_t)split * KEYS_PER_WG + (size_t)w * KEYS_PER_WAVE) * DH;
  const u16* kp = kc + base + (size_t)c * DH + r * 8;
  const u16* vp = vc + base + (size_t)(4 * r) * DH + c * 8;
  u32x4 qf[4];
  {
    const u16* qp = q + b * 4096 + (kvh * 4 + (c >> 2)) * DH + r * 8;
    for (int s = 0; s < 4; ++s) qf[s] = *(const u32x4*)(qp + 32 * s);
  }
  f32x4 S[NT];
#pragma unroll
  for (int t = 0; t < NT; ++t) {
    u32x4 kf[4];
#pragma unroll
    for (int s = 0; s < 4; ++s) kf[s] = __builtin_nontemporal_load((const u32x4*)(kp + (size_t)t * 16 * DH + 32 * s));
    f32x4 acc = {0.f, 0.f, 0.f, 0.f};
#pragma unroll
    for (int s = 0; s < 4; ++s)
      acc = __builtin_amdgcn_mfma_f32_16x16x32_bf16(__builtin_bit_cast(bf16x8_t, kf[s]), __builtin_bit_cast(bf16x8_t, qf[s]), acc, 0, 0, 0);
    S[t] = acc;
  }
  const float SCALE = 0.08838834764831845f;
  const float LOG2E = 1.4426950408889634f;
  float m = -INFINITY;
#pragma unroll
  for (int t = 0; t < NT; ++t)
#pragma unroll
    for (int i = 0; i < 4; ++i) {
      float s = rbf(rbf(S[t][i]) * rbf(SCALE));
      s = rbf(rbf(S[t][i]) * SCALE);
      S[t][i] = s; m = fmaxf(m, s);
    }
  m = fmaxf(m, __shfl_xor(m, 16)); m = fmaxf(m, __shfl_xor(m, 32));
  if (r == 0 && (c & 3) == 0) sm[w][c >> 2] = m;
  __syncthreads();
  {
    float mm = sm[0][c >> 2];
    for (int i = 1; i < NW; ++i) mm = fmaxf(mm, sm[i][c >> 2]);
    m = mm;
  }
  float l = 0.f;
  const unsigned mask0 = (c & 3) == 0 ? 0x0000ffffu : ((c & 3) == 1 ? 0xffff0000u : 0u);
  const unsigned mask1 = (c & 3) == 2 ? 0x0000ffffu : ((c & 3) == 3 ? 0xffff0000u : 0u);
  f32x4 o1 = {0.f, 0.f, 0.f, 0.f}, o2 = {0.f, 0.f, 0.f, 0.f};
#pragma unroll
  for (int t = 0; t < NT; ++t) {
    u32x4 vf[4];
#pragma unroll
    for (int i = 0; i < 4; ++i) vf[i] = __builtin_nontemporal_load((const u32x4*)(vp + (size_t)(t * 16 + i) * DH));
#pragma unroll
    for (int i = 0; i < 4; ++i) {
      float p = __builtin_amdgcn_exp2f((S[t][i] - m) * LOG2E);
      l += p;
      unsigned pb = f2bf(p); pb |= pb << 16;
      u32x2 a = {pb & mask0, pb & mask1};
      u32x2 v0 = {vf[i][0], vf[i][1]}, v1 = {vf[i][2], vf[i][3]};
      o1 = __builtin_amdgcn_mfma_f32_16x16x16bf16_1k(__builtin_bit_cast(bf16x4, a), __builtin_bit_cast(bf16x4, v0), o1, 0, 0, 0);
      o2 = __builtin_amdgcn_mfma_f32_16x16x16bf16_1k(__builtin_bit_cast(bf16x4, a), __builtin_bit_cast(bf16x4, v1), o2, 0, 0, 0);
    }
  }
  l += __shfl_xor(l, 16); l += __shfl_xor(l, 32);
  if (r == 0 && (c & 3) == 0) sl[w][c >> 2] = l;
  *(f32x4*)&so[w][r * 128 + c * 8] = o1;
  *(f32x4*)&so[w][r * 128 + c * 8 + 4] = o2;
  __syncthreads();
  {
    float s = 0.f;
    for (int i = 0; i < NW; ++i) s += so[i][tid];
    int h = tid >> 7;
    size_t slot = ((size_t)(b * HQ + kvh * 4 + h) * NSPLIT + split);
    po[slot * DH + (tid & 127)] = s;
    if ((tid & 127) == 0) {
      float L = 0.f, M = sm[0][h];
      for (int i = 0; i < NW; ++i) { L += sl[i][h]; M = fmaxf(M, sm[i][h]); }
      pml[slot * 2] = M; pml[slot * 2 + 1] = L;
    }
  }
}

__global__ void k_comb(const float* po, const float* pml, float* o) {
  int row = blockIdx.x, d = threadIdx.x;
  float M = -INFINITY;
  for (int s = 0; s < NSPLIT; ++s) M = fmaxf(M, pml[(row * NSPLIT + s) * 2]);
  float L = 0.f, a = 0.f;
  for (int s = 0; s < NSPLIT; ++s) {
    float wgt = __expf(pml[(row * NSPLIT + s) * 2] - M);
    L += wgt * pml[(row * NSPLIT + s) * 2 + 1];
    a += wgt * po[(size_t)(row * NSPLIT + s) * DH + d];
  }
  o[row * DH + d] = rbf(a / L);
}

__global__ void __launch_bounds__(1024) k_wo(const float* o, const u16* wo, const u16* x, float* h) {
  __shared__ float red[16][2][64];
  int tid = threadIdx.x, lane = tid & 63, w = tid >> 6;
  int col = blockIdx.x * 64 + lane;
  float a0 = 0.f, a1 = 0.f;
  for (int k = w * 256; k < w * 256 + 256; ++k) {
    float wv = bf2f(wo[(size_t)k * D + col]);
    a0 += o[k] * wv; a1 += o[4096 + k] * wv;
  }
  red[w][0][lane] = a0; red[w][1][lane] = a1;
  __syncthreads();
  if (w < 2) {
    float s = 0.f;
    for (int i = 0; i < 16; ++i) s += red[i][w][lane];
    h[w * D + col] = rbf(bf2f(x[w * D + col]) + rbf(s));
  }
}

__global__ void __launch_bounds__(256) k_gu(const float* h, const u16* wg, const u16* wu, float* act) {
  __shared__ float xn[2][D];
  __shared__ float red[4][4][64];
  int tid = threadIdx.x, lane = tid & 63, w = tid >> 6;
  if (w < 2) {
    float a[8], o[8];
    for (int i = 0; i < 8; ++i) a[i] = h[w * D + lane * 8 + i];
    ln_row(a, o);
    for (int i = 0; i < 8; ++i) xn[w][lane * 8 + i] = o[i];
  }
  __syncthreads();
  int col = blockIdx.x * 64 + lane;
  float g0 = 0.f, g1 = 0.f, u0 = 0.f, u1 = 0.f;
  for (int k = w * 128; k < w * 128 + 128; ++k) {
    float gv = bf2f(wg[(size_t)k * FF + col]), uv = bf2f(wu[(size_t)k * FF + col]);
    g0 += xn[0][k] * gv; g1 += xn[1][k] * gv; u0 += xn[0][k] * uv; u1 += xn[1][k] * uv;
  }
  red[w][0][lane] = g0; red[w][1][lane] = g1; red[w][2][lane] = u0; red[w][3][lane] = u1;
  __syncthreads();
  if (w < 2) {
    float g = rbf(red[0][w][lane] + red[1][w][lane] + red[2][w][lane] + red[3][w][lane]);
    float u = rbf(red[0][2 + w][lane] + red[1][2 + w][lane] + red[2][2 + w][lane] + red[3][2 + w][lane]);
    float sg = rbf(g / (1.0f + __expf(-g)));
    act[w * FF + col] = rbf(sg * u);
  }
}

__global__ void __launch_bounds__(1024) k_dn(const float* act, const u16* wd, const float* h, u16* out) {
  __shared__ float red[16][2][64];
  int tid = threadIdx.x, lane = tid & 63, w = tid >> 6;
  int col = blockIdx.x * 64 + lane;
  float a0 = 0.f, a1 = 0.f;
  for (int k = w * 112; k < w * 112 + 112; ++k) {
    float wv = bf2f(wd[(size_t)k * D + col]);
    a0 += act[k] * wv; a1 += act[FF + k] * wv;
  }
  red[w][0][lane] = a0; red[w][1][lane] = a1;
  __syncthreads();
  if (w < 2) {
    float s = 0.f;
    for (int i = 0; i < 16; ++i) s += red[i][w][lane];
    out[w * D + col] = f2bf(h[w * D + col] + rbf(s));
  }
}

extern "C" void kernel_launch(void* const* d_in, const int* in_sizes, int n_in, void* d_out, int out_size, void* d_ws, size_t ws_size, hipStream_t stream) {
  const u16* x = (const u16*)d_in[0];
  const u16* kc = (const u16*)d_in[1];
  const u16* vc = (const u16*)d_in[2];
  const u16* wq = (const u16*)d_in[3];
  const u16* wo = (const u16*)d_in[4];
  const u16* wg = (const u16*)d_in[6];
  const u16* wu = (const u16*)d_in[7];
  const u16* wd = (const u16*)d_in[8];
  char* ws = (char*)d_ws;
  u16* q = (u16*)ws;
  float* po = (float*)(ws + 65536);
  float* pml = (float*)(ws + 65536 + 524288);
  float* o = (float*)(ws + 65536 + 524288 + 65536);
  float* h = o + 8192;
  float* act = h + 1024;
  k_q<<<64, 256, 0, stream>>>(x, wq, q);
  k_attn<<<256, 512, 0, stream>>>(q, kc, vc, po, pml);
  k_comb<<<64, 128, 0, stream>>>(po, pml, o);
  k_wo<<<8, 1024, 0, stream>>>(o, wo, x, h);
  k_gu<<<28, 256, 0, stream>>>(h, wg, wu, act);
  k_dn<<<8, 1024, 0, stream>>>(act, wd, h, (u16*)d_out);
}
```

```cpp
#include <hip/hip_runtime.h>
#include <stdint.h>

#define STAMPS 0
#define PACE "8"
#define DRYRUN 0
#define NOHELP 0
#define CONTIG 0
#define WAUX AUX_NT
#define QDELAY 450
#define EARLYW 1
#define SPEC 3
#define NOQWAIT 0
#define WIN 6
#define WCNT 44
#define BAL 1

typedef unsigned short u16;
typedef unsigned long long u64;
typedef unsigned u32x4 __attribute__((ext_vector_type(4)));
typedef unsigned u32x2 __attribute__((ext_vector_type(2)));
typedef float f32x4 __attribute__((ext_vector_type(4)));
typedef __bf16 bf16x8_t __attribute__((ext_vector_type(8)));
typedef __bf16 bf16x2_t __attribute__((ext_vector_type(2)));
typedef float f32x2 __attribute__((ext_vector_type(2)));
typedef short s16x4 __attribute__((ext_vector_type(4)));
#define GAS __attribute__((address_space(1)))
#define LAS __attribute__((address_space(3)))

constexpr int DM = 512, SK = 32768, FF = 1792;
constexpr int NWG = 256, NTHR = 512;
constexpr int AUX_NT = 2, AUX_SC1 = 16;
constexpr int RSRC3 = 0x00020000;
constexpr unsigned SPIN_CAP = 1u << 18;

constexpr int WS_EPOCH = 0;
constexpr int WS_Q = 4096;
constexpr int WS_HDR = WS_Q + 32768;
constexpr int WS_REC = WS_HDR + 8192;
constexpr int WS_HP = WS_REC + 524288;
constexpr int WS_DP = WS_HP + 65536;
constexpr int WS_OUTD = WS_DP + 56 * 8192;
constexpr int WS_SPAN = 2 << 20;
constexpr int WS_STAMP = 4 << 20;
static_assert(WS_OUTD + 4096 <= WS_SPAN, "ws map");

constexpr int L_SLAB = 0;
constexpr int L_Q = 98304;
constexpr int L_A = L_Q + 1024;
constexpr int L_ACT = L_A + 2176;
constexpr int L_MRG = L_ACT + 128;
constexpr int L_RED = L_MRG + 4 * 2560;
constexpr int L_CNT = L_RED + 64;
constexpr int L_BYTES = L_CNT + 64;
constexpr int C_QRDY = 0, C_TAG = 1, C_MRG = 2, C_HS = 3, C_GO = 4;
constexpr int ASTR = 1088;

struct Params {
  const u16 *x, *kc, *vc, *wq, *wo, *wg, *wu, *wd;
  u16* out;
  unsigned char* ws;
};

__device__ __forceinline__ float bf_lo(unsigned u) { return __builtin_bit_cast(float, u << 16); }
__device__ __forceinline__ float bf_hi(unsigned u) { return __builtin_bit_cast(float, u & 0xffff0000u); }
__device__ __forceinline__ unsigned pk_bf(float lo, float hi) {
  bf16x2_t r = __builtin_convertvector(f32x2{lo, hi}, bf16x2_t);
  return __builtin_bit_cast(unsigned, r);
}
__device__ __forceinline__ float rbf(float f) { return bf_lo(pk_bf(f, f)); }
template <int CTRL> __device__ __forceinline__ float dpp(float x) {
  return __builtin_bit_cast(float, __builtin_amdgcn_mov_dpp(__builtin_bit_cast(int, x), CTRL, 0xf, 0xf, true));
}
constexpr int XOR1 = 0xB1, XOR2 = 0x4E, HMIR = 0x141, MIR = 0x140, ROR8 = 0x128;
__device__ __forceinline__ float x16_32_max(float x) {
  auto s = __builtin_amdgcn_permlane16_swap(__float_as_uint(x), __float_as_uint(x), false, false);
  x = fmaxf(__uint_as_float(s[0]), __uint_as_float(s[1]));
  auto t = __builtin_amdgcn_permlane32_swap(__float_as_uint(x), __float_as_uint(x), false, false);
  return fmaxf(__uint_as_float(t[0]), __uint_as_float(t[1]));
}
__device__ __forceinline__ float x16_32_sum(float x) {
  auto s = __builtin_amdgcn_permlane16_swap(__float_as_uint(x), __float_as_uint(x), false, false);
  x = __uint_as_float(s[0]) + __uint_as_float(s[1]);
  auto t = __builtin_amdgcn_permlane32_swap(__float_as_uint(x), __float_as_uint(x), false, false);
  return __uint_as_float(t[0]) + __uint_as_float(t[1]);
}
__device__ __forceinline__ float row_sum(float v) { v += dpp<XOR1>(v); v += dpp<XOR2>(v); v += dpp<HMIR>(v); v += dpp<MIR>(v); return v; }
__device__ __forceinline__ float row_max(float v) { v = fmaxf(v, dpp<XOR1>(v)); v = fmaxf(v, dpp<XOR2>(v)); v = fmaxf(v, dpp<HMIR>(v)); v = fmaxf(v, dpp<MIR>(v)); return v; }
__device__ __forceinline__ float wave_sum(float v) { return x16_32_sum(row_sum(v)); }
__device__ __forceinline__ f32x4 mfma32(u32x4 a, u32x4 b, f32x4 c) {
  return __builtin_amdgcn_mfma_f32_16x16x32_bf16(__builtin_bit_cast(bf16x8_t, a), __builtin_bit_cast(bf16x8_t, b), c, 0, 0, 0);
}
__device__ __forceinline__ f32x4 mfma16(u32x2 a, u32x2 b, f32x4 c) {
  return __builtin_amdgcn_mfma_f32_16x16x16bf16_1k(__builtin_bit_cast(s16x4, a), __builtin_bit_cast(s16x4, b), c, 0, 0, 0);
}
#define MEMBAR() asm volatile("" ::: "memory")
#define WAIT_LDS() asm volatile("s_waitcnt lgkmcnt(0)" ::: "memory")
#define WAIT_VM() asm volatile("s_waitcnt vmcnt(0)" ::: "memory")

#define LD4X(C, d, off, rs, st) asm volatile("buffer_load_dwordx4 %0, %4, %5, 0 offen nt\n\tbuffer_load_dwordx4 %1, %4, %5, 0 offen offset:" #st " nt\n\t" \
    "buffer_load_dwordx4 %2, %4, %5, 0 offen offset:2*" #st " nt\n\tbuffer_load_dwordx4 %3, %4, %5, 0 offen offset:3*" #st " nt" \
    : C(d[0]), C(d[1]), C(d[2]), C(d[3]) : "v"(off), "s"(rs))
#define LD4(d, off, rs, st) LD4X("+v", d, off, rs, st)
#define LD4I(d, off, rs, st) LD4X("=&v", d, off, rs, st)
#define WAIT4(d, n) asm volatile("s_waitcnt vmcnt(" #n ")" : "+v"(d[0]), "+v"(d[1]), "+v"(d[2]), "+v"(d[3]))
#define WAIT4X(d, n) WAIT4(d, n)
__device__ __forceinline__ u32x4 make_srd(const void* base, unsigned bytes) {
  const u64 a = (u64)base;
  return u32x4{(unsigned)a, (unsigned)(a >> 32) & 0xffffu, bytes, (unsigned)RSRC3};
}
#define POLL2(d, off, rs, so) asm volatile("buffer_load_dwordx2 %0, %1, %2, %3 offen sc1" : "+v"(d) : "v"(off), "s"(rs), "s"(so) : "memory")
#define POLL4(d, off, rs, so) asm volatile("buffer_load_dwordx4 %0, %1, %2, %3 offen sc1" : "+v"(d) : "v"(off), "s"(rs), "s"(so) : "memory")
#define PIN4(a, b, c, d) asm volatile("" : "+v"(a), "+v"(b), "+v"(c), "+v"(d))

#define STAMP(i) do { } while (0)
#define STAMPC(i) do { } while (0)
#define DUMP() do { } while (0)

#define DMA16(src, dst) __builtin_amdgcn_global_load_lds((const GAS unsigned*)(src), (LAS unsigned*)(dst), 16, 0, WAUX)
__device__ __forceinline__ u32x4 tr_frag(LAS unsigned char* a, int plus4) {
  const u32x2 lo = __builtin_bit_cast(u32x2, __builtin_amdgcn_ds_read_tr16_b64_v4i16((LAS s16x4*)a));
  const u32x2 hi = __builtin_bit_cast(u32x2, __builtin_amdgcn_ds_read_tr16_b64_v4i16((LAS s16x4*)(a + plus4)));
  return u32x4{lo[0], lo[1], hi[0], hi[1]};
}
__device__ __forceinline__ f32x4 gemv16(LAS unsigned char* avec, int lane, const u32x4 (&bf)[16]) {
  LAS unsigned char* a = avec + (lane & 1) * ASTR + (lane >> 4) * 16;
  u32x4 af[16];
#pragma unroll
  for (int s = 0; s < 16; ++s) af[s] = *(LAS u32x4*)(a + s * 64);
  f32x4 d0 = {0.f, 0.f, 0.f, 0.f}, d1 = {0.f, 0.f, 0.f, 0.f};
#pragma unroll
  for (int s = 0; s < 16; s += 2) { d0 = mfma32(af[s], bf[s], d0); d1 = mfma32(af[s + 1], bf[s + 1], d1); }
  return d0 + d1;
}
__device__ __forceinline__ void lds_wait_ge(volatile LAS unsigned* w, unsigned v) {
#pragma unroll 1
  for (unsigned sp = 0; sp < (SPIN_CAP << 4); ++sp) { if ((unsigned)__builtin_amdgcn_readfirstlane(*w) >= v) break; __builtin_amdgcn_s_sleep(1); }
  MEMBAR();
}
#define LIVE_BARRIER() do { WAIT_LDS(); if (live) __builtin_amdgcn_s_barrier(); MEMBAR(); } while (0)

__global__ void __launch_bounds__(NTHR, 2) fused_decode(Params p) {
  extern __shared__ __attribute__((aligned(16))) unsigned char smem[];
  LAS unsigned char* L = (LAS unsigned char*)smem;
  volatile LAS unsigned* ctl = (volatile LAS unsigned*)(L + L_CNT);
  const int g = blockIdx.x, tid = threadIdx.x, lane = tid & 63, w = __builtin_amdgcn_readfirstlane(tid >> 6);
  const int kvh = g & 7, bat = g >> 7, split = (g >> 3) & 15;
  const __amdgpu_buffer_rsrc_t rsw = __builtin_amdgcn_make_buffer_rsrc((void*)p.ws, 0, 2 * WS_SPAN, RSRC3);
  const float LOG2E = 1.4426950408889634f;

  if (w < 4) {
    if (w == 0) { STAMP(0); STAMPC(20); }
    const int r = lane >> 4, c = lane & 15;
    int u0, nu;
    if (BAL) {
      if (split < 8) { u0 = 127 * split; nu = 127; }
      else if (bat == 0) { if (split < 12) { u0 = 1016 + 123 * (split - 8); nu = 123; } else { u0 = 1508 + 135 * (split - 12); nu = 135; } }
      else { if (split < 11) { u0 = 1016 + 123 * (split - 8); nu = 123; } else if (split < 14) { u0 = 1385 + 133 * (split - 11); nu = 133; } else { u0 = 1784 + 132 * (split - 14); nu = 132; } }
    } else { u0 = 128 * split; nu = 128; }
    const size_t base = ((size_t)(bat * 8 + kvh) * SK + (size_t)u0 * 16) * 256;
    const u32x4 rsk = make_srd((const unsigned char*)p.kc + base, nu * 4096), rsv = make_srd((const unsigned char*)p.vc + base, nu * 4096);
    int rem = (nu - w + 3) >> 2;
    int koff = w * 4096 + c * 256 + r * 16, voff = w * 4096 + r * 1024 + c * 16;
    constexpr int W = WIN;
    u32x4 kb[W][4], vb[W][4];
    __builtin_amdgcn_s_barrier();
    __builtin_amdgcn_sched_barrier(0);
#pragma unroll
    for (int j = 0; j < W; ++j) {
      LD4I(kb[j], koff, rsk, 64);
      if (j > 0) asm volatile("s_waitcnt vmcnt(" PACE ")");
      LD4I(vb[j], voff, rsv, 256);
      if (j > 0) asm volatile("s_waitcnt vmcnt(" PACE ")");
      koff += 16384; voff += 16384;
    }
    __builtin_amdgcn_sched_barrier(0);
    lds_wait_ge(ctl + C_QRDY, 1u);
    u32x4 qf[4];
#pragma unroll
    for (int s = 0; s < 4; ++s) qf[s] = *(LAS u32x4*)(L + L_Q + (c >> 2) * 256 + s * 64 + r * 16);
    if (w == 0) STAMP(1);
    const unsigned mask0 = (c & 3) == 0 ? 0x0000ffffu : ((c & 3) == 1 ? 0xffff0000u : 0u);
    const unsigned mask1 = (c & 3) == 2 ? 0x0000ffffu : ((c & 3) == 3 ? 0xffff0000u : 0u);
    const float SCALE = 0.08838834764831845f;
    float mL = -1.0e38f, l = 0.f;
    f32x4 o1 = {0.f, 0.f, 0.f, 0.f}, o2 = {0.f, 0.f, 0.f, 0.f};
#pragma unroll 1
    for (;;) {
#pragma unroll
      for (int j = 0; j < W; ++j) {
        f32x4 S = {0.f, 0.f, 0.f, 0.f};
        WAIT4X(kb[j], WCNT);
#pragma unroll
        for (int s = 0; s < 4; ++s) S = mfma32(kb[j][s], qf[s], S);
        LD4(kb[j], koff, rsk, 64);
        koff += 16384;
        const unsigned a01 = pk_bf(S[0], S[1]), a23 = pk_bf(S[2], S[3]);
        const unsigned b01 = pk_bf(bf_lo(a01) * SCALE, bf_hi(a01) * SCALE), b23 = pk_bf(bf_lo(a23) * SCALE, bf_hi(a23) * SCALE);
        const float s0 = bf_lo(b01), s1 = bf_hi(b01), s2 = bf_lo(b23), s3 = bf_hi(b23);
        const float mx = x16_32_max(fmaxf(fmaxf(s0, s1), fmaxf(s2, s3)));
        const float mn = fmaxf(mL, mx * LOG2E);
        const float alpha = __builtin_amdgcn_exp2f(mL - mn);
        mL = mn;
        float p0 = __builtin_amdgcn_exp2f(fmaf(s0, LOG2E, -mn)), p1 = __builtin_amdgcn_exp2f(fmaf(s1, LOG2E, -mn));
        float p2 = __builtin_amdgcn_exp2f(fmaf(s2, LOG2E, -mn)), p3 = __builtin_amdgcn_exp2f(fmaf(s3, LOG2E, -mn));
        l = fmaf(l, alpha, (p0 + p1) + (p2 + p3));
        o1 *= alpha; o2 *= alpha;
        PIN4(p0, p1, p2, p3);
        WAIT4X(vb[j], WCNT);
        const float pp[4] = {p0, p1, p2, p3};
#pragma unroll
        for (int i = 0; i < 4; ++i) {
          const unsigned pb = pk_bf(pp[i], pp[i]);
          const u32x2 bp = {pb & mask0, pb & mask1};
          o1 = mfma16(u32x2{vb[j][i][0], vb[j][i][1]}, bp, o1);
          o2 = mfma16(u32x2{vb[j][i][2], vb[j][i][3]}, bp, o2);
        }
        if (--rem == 0) goto stream_done;
        LD4(vb[j], voff, rsv, 256);
        voff += 16384;
      }
    }
  stream_done:
    l = x16_32_sum(l);
    LAS float* mg = (LAS float*)(L + L_MRG) + lane;
#pragma unroll
    for (int i = 0; i < 4; ++i) { mg[w * 640 + i * 64] = o1[i]; mg[w * 640 + (4 + i) * 64] = o2[i]; }
    mg[w * 640 + 512] = mL; mg[w * 640 + 576] = l;
    WAIT_LDS();
    unsigned old = 0;
    if (lane == 0) old = __hip_atomic_fetch_add((LAS unsigned*)(L + L_CNT) + C_MRG, 1u, __ATOMIC_RELAXED, __HIP_MEMORY_SCOPE_WORKGROUP);
    old = __builtin_amdgcn_readfirstlane(old);
    MEMBAR();
    if (old == 3) {
      const unsigned tag = __builtin_amdgcn_readfirstlane(ctl[C_TAG]);
      float mw[4], M, Lt = 0.f, acc[8];
#pragma unroll
      for (int u = 0; u < 4; ++u) mw[u] = mg[u * 640 + 512];
      M = fmaxf(fmaxf(mw[0], mw[1]), fmaxf(mw[2], mw[3]));
#pragma unroll
      for (int i = 0; i < 8; ++i) acc[i] = 0.f;
#pragma unroll
      for (int u = 0; u < 4; ++u) {
        const float f = __builtin_amdgcn_exp2f(mw[u] - M);
        Lt = fmaf(f, mg[u * 640 + 576], Lt);
#pragma unroll
        for (int i = 0; i < 8; ++i) acc[i] = fmaf(f, mg[u * 640 + i * 64], acc[i]);
      }
      const float inv = __builtin_amdgcn_rcpf(Lt);
      const int rec = bat * 16 + split, h = c >> 2;
      const int ro = WS_REC + (kvh * 32 + rec) * 2048 + h * 512 + (r * 16 + (c & 3) * 4) * 8;
#pragma unroll
      for (int i = 0; i < 4; i += 2)
        __builtin_amdgcn_raw_buffer_store_b128(u32x4{pk_bf(acc[i] * inv, acc[4 + i] * inv), tag, pk_bf(acc[i + 1] * inv, acc[5 + i] * inv), tag}, rsw, ro + i * 8, 0, AUX_SC1);
      if (r == 0 && (c & 3) == 0)
        __builtin_amdgcn_raw_buffer_store_b64(u32x2{__float_as_uint(M + __builtin_amdgcn_logf(Lt)), tag}, rsw, WS_HDR + ((kvh * 4 + h) * 32 + rec) * 8, 0, AUX_SC1);
      STAMP(3); STAMPC(21);
    }
#pragma unroll
    for (int j = 0; j < W; ++j) { WAIT4(kb[j], 0); WAIT4(vb[j], 0); }
    DUMP();
    return;
  }

  __builtin_amdgcn_s_setprio(3);
  const u64 t_start = __builtin_amdgcn_s_memrealtime();
  const int u = w - 4;
  const int sub = bat * 4 + (split & 3);
  const int job = (split >> 2) == 2 && sub == 7 ? 4 : (split >> 2);
  const int fj = 7 * kvh + sub;
  const int rho = sub * 8 + kvh;
  LAS unsigned char* slab = L + L_SLAB;
  u32x4 bf[16], bd[8];
  unsigned ep = 0;
  u32x4 xv[2];
  unsigned xr[2] = {0u, 0u};
  if (u == 0) ep = __builtin_amdgcn_raw_buffer_load_b32(rsw, WS_EPOCH, 0, AUX_SC1);
  auto stage = [&]() {
    if (job == 1) {
      const unsigned char* src = (const unsigned char*)p.wo + (size_t)(kvh * 512 + u * 128 + 4 * (lane >> 3)) * 1024 + sub * 128 + (lane & 7) * 16;
#pragma unroll
      for (int i = 0; i < 16; ++i) DMA16(src + (32 * (i >> 2) + (i & 3)) * 1024, slab + (u * 16 + i) * 1024);
    } else if (job == 2) {
#pragma unroll
      for (int b = 0; b < 2; ++b) xr[b] = *(const GAS unsigned*)((const GAS unsigned char*)p.x + b * 1024 + (u * 128 + 2 * lane) * 2);
      const unsigned char* src = (const unsigned char*)((lane & 4) ? p.wu : p.wg) + (size_t)(u * 128 + (lane >> 3)) * (FF * 2) + fj * 64 + (lane & 3) * 16;
#pragma unroll
      for (int i = 0; i < 16; ++i) DMA16(src + (size_t)i * 8 * FF * 2, slab + (u * 16 + i) * 1024);
      const unsigned char* srd = (const unsigned char*)p.wd + (size_t)(fj * 32 + u * 8) * 1024 + lane * 16;
#pragma unroll
      for (int i = 0; i < 8; ++i) DMA16(srd + i * 1024, slab + 65536 + (u * 8 + i) * 1024);
    } else if (job == 3) {
      xr[0] = *(const GAS u16*)((const GAS unsigned char*)p.x + (rho >> 5) * 1024 + ((rho & 31) * 16 + (lane & 15)) * 2);
    }
  };
  if (job == 0 && NOHELP != 1) {
    if (u == 0) {
#pragma unroll
      for (int b = 0; b < 2; ++b) xv[b] = *(const GAS u32x4*)((const GAS unsigned char*)p.x + b * 1024 + lane * 16);
    }
    const unsigned char* src = (const unsigned char*)p.wq + (size_t)(u * 128 + (lane >> 3)) * 8192 + (kvh * 512 + sub * 64) * 2;
    const int pc = (lane & 7) ^ ((lane >> 3) & 2);
#pragma unroll
    for (int i = 0; i < 16; ++i) DMA16(src + (size_t)i * 8 * 8192 + (pc ^ ((i & 1) << 2)) * 16, slab + (u * 16 + i) * 1024);
  }
  if (EARLYW && NOHELP != 1 && NOHELP != 2) stage();
  if (u == 0) { if (lane < 16) ctl[lane] = 0u; WAIT_LDS(); }
  __builtin_amdgcn_sched_barrier(0);
  __builtin_amdgcn_s_barrier();
  __builtin_amdgcn_sched_barrier(0);
  if (NOHELP == 1) { DUMP(); return; }
  LAS unsigned char* fa = slab + ((lane >> 4) * 8 + ((lane >> 2) & 3)) * 128;
  unsigned tag = 0;
  if (u == 0) {
    tag = __builtin_amdgcn_readfirstlane(ep) + 1u;
    ctl[C_TAG] = tag;
  }
  if (job == 0) {
    if (u == 0) {
#pragma unroll
      for (int b = 0; b < 2; ++b) {
        float v[8];
#pragma unroll
        for (int i = 0; i < 4; ++i) { v[2 * i] = bf_lo(xv[b][i]); v[2 * i + 1] = bf_hi(xv[b][i]); }
        float s = 0.f;
#pragma unroll
        for (int i = 0; i < 8; ++i) s += v[i];
        const float mean = wave_sum(s) * (1.0f / DM);
        float q2 = 0.f;
#pragma unroll
        for (int i = 0; i < 8; ++i) q2 = fmaf(v[i] - mean, v[i] - mean, q2);
        const float var = wave_sum(q2) * (1.0f / DM);
        const float mb = rbf(mean), rd = __builtin_amdgcn_rcpf(rbf(__builtin_amdgcn_sqrtf(rbf(rbf(var) + 1e-5f))));
        u32x4 o;
#pragma unroll
        for (int i = 0; i < 4; ++i) o[i] = pk_bf(rbf(v[2 * i] - mb) * rd, rbf(v[2 * i + 1] - mb) * rd);
        *(LAS u32x4*)(L + L_A + b * ASTR + lane * 16) = o;
      }
    }
    WAIT_VM();
    WAIT_LDS();
    if (lane == 0) __hip_atomic_fetch_add((LAS unsigned*)(L + L_CNT) + C_HS, 1u, __ATOMIC_RELAXED, __HIP_MEMORY_SCOPE_WORKGROUP);
    lds_wait_ge(ctl + C_HS, 4u);
    if (u == 0) STAMP(12);
    tag = __builtin_amdgcn_readfirstlane(ctl[C_TAG]);
    const int sw = ((lane >> 3) & 3) ^ u;
#pragma unroll
    for (int s = 0; s < 16; ++s) bf[s] = tr_frag(fa + sw * 32 + (lane & 3) * 8 + s * 4096, 512);
    const f32x4 d = gemv16(L + L_A, lane, bf);
    const float v = (lane & 16) ? d[1] : d[0];
    const float nb = dpp<XOR1>(v);
    if (lane < 32 && !(lane & 1))
      __builtin_amdgcn_raw_buffer_store_b64(u32x2{pk_bf(v, nb), tag}, rsw, WS_Q + ((lane >> 4) * 2048 + kvh * 256 + sub * 32 + u * 8 + ((lane & 15) >> 1)) * 8, 0, AUX_SC1);
    if (u == 0) STAMP(4);
  }
  if (u == 0) {
    const int qo = WS_Q + (bat * 2048 + kvh * 256 + lane * 2) * 8;
    u32x4 gq[2];
#pragma unroll 1
    for (unsigned sp = 0; sp < 4096u && __builtin_amdgcn_s_memrealtime() - t_start < QDELAY; ++sp) __builtin_amdgcn_s_sleep(2);
#pragma unroll 1
    for (unsigned sp = 0; sp < SPIN_CAP; ++sp) {
      MEMBAR();
#pragma unroll
      for (int i = 0; i < 2; ++i) gq[i] = __builtin_amdgcn_raw_buffer_load_b128(rsw, qo + i * 1024, 0, AUX_SC1);
      const bool ok = gq[0][1] == tag && gq[0][3] == tag && gq[1][1] == tag && gq[1][3] == tag;
      if (__all(ok)) break;
      __builtin_amdgcn_s_sleep(8);
    }
#pragma unroll
    for (int i = 0; i < 2; ++i) *(LAS u32x2*)(L + L_Q + (i * 128 + lane * 2) * 4) = u32x2{gq[i][0], gq[i][2]};
    WAIT_LDS();
    ctl[C_QRDY] = 1u;
    STAMP(5);
  }
  if (NOHELP == 2) { DUMP(); return; }
  if (job == 0 || job == 4 || (job == 3 && u != 0)) { DUMP(); return; }
  if (u != 0) {
    lds_wait_ge(ctl + C_QRDY, 1u);
    tag = __builtin_amdgcn_readfirstlane(ctl[C_TAG]);
  }
  if (!EARLYW) stage();

  if (job != 3) {
    WAIT_VM();
    if (lane == 0) __hip_atomic_fetch_add((LAS unsigned*)(L + L_CNT) + C_HS, 1u, __ATOMIC_RELAXED, __HIP_MEMORY_SCOPE_WORKGROUP);
    lds_wait_ge(ctl + C_HS, 4u);
    const int co = job == 1 ? u * 32 + (lane & 3) * 8 : (lane & 2) * 32 + u * 16 + (lane & 1) * 8;
#pragma unroll
    for (int s = 0; s < 16; ++s) bf[s] = tr_frag(fa + co + s * 4096, 512);
    LAS unsigned char* fd = slab + 65536 + ((lane >> 4) * 8 + ((lane >> 2) & 3)) * 1024 + u * 256 + (lane & 3) * 8;
#pragma unroll
    for (int t = 0; t < 8; ++t) bd[t] = tr_frag(fd + t * 32, 4096);
  }
  if (NOHELP == 3) { asm volatile("" :: "v"(bf[0]), "v"(bd[0])); DUMP(); return; }
#pragma unroll 1
  for (int pass = DRYRUN ? 0 : 1; pass < 2; ++pass) {
    int live = pass;
    asm volatile("" : "+s"(live));
    const u32x4 rsp = make_srd(p.ws, live ? WS_SPAN : 0);
    const __amdgpu_buffer_rsrc_t rsx = __builtin_amdgcn_make_buffer_rsrc((void*)p.ws, 0, live ? WS_SPAN : 0, RSRC3);
    const int ho = WS_HDR + ((kvh * 4 + u) * 32 + (lane & 31)) * 8;
    if (job == 1) {
      const int ro = WS_REC + kvh * 65536 + u * 512 + lane * 8;
      u32x2 hd = {0u, 0u}, gr[32];
#pragma unroll
      for (int s = 0; s < 32; ++s) gr[s] = u32x2{0u, 0u};
      unsigned pend = 0xffu, want = live ? 0u : 0xffu;
#pragma unroll 1
      for (unsigned sp = 0; sp < SPIN_CAP; ++sp) {
        POLL2(hd, ho, rsp, 0);
#pragma unroll
        for (int q = 0; q < 8; ++q)
          if (want & (1u << q)) {
#pragma unroll
            for (int s = 4 * q; s < 4 * q + 4; ++s) POLL2(gr[s], ro, rsp, s * 2048);
          }
        asm volatile("s_waitcnt vmcnt(0)" : "+v"(hd) :: "memory");
#pragma unroll
        for (int q = 0; q < 8; ++q)
          if (want & (1u << q)) {
            PIN4(gr[4 * q], gr[4 * q + 1], gr[4 * q + 2], gr[4 * q + 3]);
            if (__all(gr[4 * q][1] == tag && gr[4 * q + 1][1] == tag && gr[4 * q + 2][1] == tag && gr[4 * q + 3][1] == tag)) pend &= ~(1u << q);
          }
        unsigned hm = (unsigned)__ballot(hd[1] == tag);
        if ((pend == 0 && hm == 0xffffffffu) || !live) break;
        const int miss = 32 - __builtin_popcount(hm);
        if (miss <= SPEC) want = pend;
        else {
          hm &= hm >> 1; hm &= hm >> 2;
          unsigned rdy = 0;
#pragma unroll
          for (int q = 0; q < 8; ++q) rdy |= ((hm >> (4 * q)) & 1u) << q;
          want = pend & rdy;
          if (miss == 32) __builtin_amdgcn_s_sleep(32); else __builtin_amdgcn_s_sleep(4);
        }
      }
      if (u == 0 && live) STAMP(6);
      const float lse = __uint_as_float(hd[0]);
      const float e = __builtin_amdgcn_exp2f(lse - row_max(lse));
      const float wn = e * __builtin_amdgcn_rcpf(row_sum(e));
      float a[4] = {0.f, 0.f, 0.f, 0.f};
#pragma unroll
      for (int s = 0; s < 32; ++s) {
        const float ws = __builtin_bit_cast(float, __builtin_amdgcn_readlane(__builtin_bit_cast(int, wn), s));
        a[(s >> 4) * 2] = fmaf(ws, bf_lo(gr[s][0]), a[(s >> 4) * 2]);
        a[(s >> 4) * 2 + 1] = fmaf(ws, bf_hi(gr[s][0]), a[(s >> 4) * 2 + 1]);
      }
      *(LAS unsigned*)(L + L_A + (u * 64 + lane) * 4) = pk_bf(a[0], a[1]);
      *(LAS unsigned*)(L + L_A + ASTR + (u * 64 + lane) * 4) = pk_bf(a[2], a[3]);
      LIVE_BARRIER();
      if (u == 0 && live) STAMP(13);
      const f32x4 d = gemv16(L + L_A, lane, bf);
      const float v = (lane & 16) ? d[1] : d[0];
      if (lane < 32)
        __builtin_amdgcn_raw_buffer_store_b64(u32x2{__float_as_uint(v), tag}, rsx, WS_HP + ((kvh * 2 + (lane >> 4)) * 512 + sub * 64 + u * 16 + (lane & 15)) * 8, 0, AUX_SC1);
      if (u == 0 && live) STAMP(7);
    } else {
      if (live) {
        if (u == 0) {
          lds_wait_ge(ctl + C_MRG, 4u);
#pragma unroll 1
          for (unsigned sp = 0; sp < SPIN_CAP; ++sp) {
            MEMBAR();
            const u32x2 t = __builtin_amdgcn_raw_buffer_load_b64(rsx, ho, 0, AUX_SC1);
            if (__all(t[1] == tag)) break;
            __builtin_amdgcn_s_sleep(6);
          }
          ctl[C_GO] = 1u;
        } else lds_wait_ge(ctl + C_GO, 1u);
      }
      if (job == 2) {
        const int po = WS_HP + (u * 128 + 2 * lane) * 8;
        u32x4 gp[16];
#pragma unroll
        for (int s = 0; s < 16; ++s) gp[s] = u32x4{0u, 0u, 0u, 0u};
        unsigned pend = 0xffu;
#pragma unroll 1
        for (unsigned sp = 0; sp < SPIN_CAP; ++sp) {
#pragma unroll
          for (int q = 0; q < 8; ++q)
            if (pend & (1u << q)) { POLL4(gp[2 * q], po, rsp, q * 8192); POLL4(gp[2 * q + 1], po, rsp, q * 8192 + 4096); }
          WAIT_VM();
#pragma unroll
          for (int q = 0; q < 8; ++q)
            if (pend & (1u << q)) {
              asm volatile("" : "+v"(gp[2 * q]), "+v"(gp[2 * q + 1]));
              if (__all(gp[2 * q][1] == tag && gp[2 * q][3] == tag && gp[2 * q + 1][1] == tag && gp[2 * q + 1][3] == tag)) pend &= ~(1u << q);
            }
          if (pend == 0 || !live) break;
        }
        if (u == 0 && live) STAMP(8);
        float h[2][2], mw[2], m2[2];
#pragma unroll
        for (int b = 0; b < 2; ++b) {
          float s0 = 0.f, s1 = 0.f;
#pragma unroll
          for (int k = 0; k < 8; ++k) { s0 += __uint_as_float(gp[2 * k + b][0]); s1 += __uint_as_float(gp[2 * k + b][2]); }
          h[b][0] = rbf(bf_lo(xr[b]) + rbf(s0)); h[b][1] = rbf(bf_hi(xr[b]) + rbf(s1));
          mw[b] = wave_sum(h[b][0] + h[b][1]) * (1.0f / 128);
          const float d0 = h[b][0] - mw[b], d1 = h[b][1] - mw[b];
          m2[b] = wave_sum(fmaf(d0, d0, d1 * d1));
        }
        if (lane == 0) *(LAS f32x4*)(L + L_RED + u * 16) = f32x4{mw[0], m2[0], mw[1], m2[1]};
        LIVE_BARRIER();
        if (u == 0 && live) STAMP(15);
        f32x4 st[4];
#pragma unroll
        for (int k = 0; k < 4; ++k) st[k] = *(LAS f32x4*)(L + L_RED + k * 16);
#pragma unroll
        for (int b = 0; b < 2; ++b) {
          const float mean = 0.25f * ((st[0][2 * b] + st[1][2 * b]) + (st[2][2 * b] + st[3][2 * b]));
          float ss = (st[0][2 * b + 1] + st[1][2 * b + 1]) + (st[2][2 * b + 1] + st[3][2 * b + 1]);
#pragma unroll
          for (int k = 0; k < 4; ++k) { const float dm = st[k][2 * b] - mean; ss = fmaf(128.0f * dm, dm, ss); }
          const float var = ss * (1.0f / DM);
          const float mb = rbf(mean), rd = __builtin_amdgcn_rcpf(rbf(__builtin_amdgcn_sqrtf(rbf(rbf(var) + 1e-5f))));
          *(LAS unsigned*)(L + L_A + b * ASTR + (u * 64 + lane) * 4) = pk_bf(rbf(h[b][0] - mb) * rd, rbf(h[b][1] - mb) * rd);
        }
        LIVE_BARRIER();
        if (u == 0 && live) STAMP(14);
        const f32x4 d = gemv16(L + L_A, lane, bf);
        const float gv = rbf((lane & 16) ? d[1] : d[0]);
        const float uv = dpp<ROR8>(gv);
        const float sg = rbf(gv * __builtin_amdgcn_rcpf(1.0f + __builtin_amdgcn_exp2f(-gv * LOG2E)));
        const float av = sg * uv;
        const float nb = dpp<XOR1>(av);
        if (lane < 32 && (lane & 9) == 0) *(LAS unsigned*)(L + L_ACT + (lane >> 4) * 64 + u * 16 + (lane & 7) * 2) = pk_bf(av, nb);
        LIVE_BARRIER();
        if (u == 0 && live) STAMP(16);
        const u32x4 aa = *(LAS u32x4*)(L + L_ACT + (lane & 1) * 64 + (lane >> 4) * 16);
        const int dpo = WS_DP + (fj * 1024 + ((lane >> 4) & 1) * 512 + u * 128 + (lane >> 5) * 16 + (lane & 15)) * 8;
#pragma unroll
        for (int t = 0; t < 8; t += 2) {
          const f32x4 e0 = mfma32(aa, bd[t], f32x4{0.f, 0.f, 0.f, 0.f}), e1 = mfma32(aa, bd[t + 1], f32x4{0.f, 0.f, 0.f, 0.f});
          const float v0 = (lane & 16) ? e0[1] : e0[0], v1 = (lane & 16) ? e1[1] : e1[0];
          __builtin_amdgcn_raw_buffer_store_b64(u32x2{__float_as_uint((lane & 32) ? v1 : v0), tag}, rsx, dpo + t * 128, 0, AUX_SC1);
        }
        if (u == 0 && live) STAMP(9);
      } else {
        const int co = (rho >> 5) * 512 + (rho & 31) * 16 + (lane & 15);
        u32x2 gp[8];
#pragma unroll 1
        for (unsigned sp = 0; sp < SPIN_CAP; ++sp) {
          MEMBAR();
#pragma unroll
          for (int s = 0; s < 8; ++s) gp[s] = __builtin_amdgcn_raw_buffer_load_b64(rsx, WS_HP + co * 8 + s * 8192, 0, AUX_SC1);
          bool ok = true;
#pragma unroll
          for (int s = 0; s < 8; ++s) ok = ok && gp[s][1] == tag;
          if (__all(ok) || !live) break;
          __builtin_amdgcn_s_sleep(4);
        }
        float s0 = 0.f;
#pragma unroll
        for (int s = 0; s < 8; ++s) s0 += __uint_as_float(gp[s][0]);
        const float hres = rbf(bf_lo(xr[0]) + rbf(s0));
        u32x2 ga[14];
        const int ao = WS_DP + ((lane >> 4) * 1024 + co) * 8;
#pragma unroll 1
        for (unsigned sp = 0; sp < SPIN_CAP; ++sp) {
          MEMBAR();
#pragma unroll
          for (int i = 0; i < 14; ++i) ga[i] = __builtin_amdgcn_raw_buffer_load_b64(rsx, ao + i * 32768, 0, AUX_SC1);
          bool ok = true;
#pragma unroll
          for (int i = 0; i < 14; ++i) ok = ok && ga[i][1] == tag;
          if (__all(ok) || !live) break;
        }
        if (live) STAMP(10);
        float y = 0.f;
#pragma unroll
        for (int i = 0; i < 14; ++i) y += __uint_as_float(ga[i][0]);
        y = x16_32_sum(y);
        const unsigned ob = pk_bf(hres + rbf(y), 0.f);
        const __amdgpu_buffer_rsrc_t rso = __builtin_amdgcn_make_buffer_rsrc((void*)p.out, 0, live ? 2048 : 0, RSRC3);
        if (lane < 16) __builtin_amdgcn_raw_buffer_store_b16((u16)ob, rso, co * 2, 0, 0);
        if (rho == 0 && lane == 0) __builtin_amdgcn_raw_buffer_store_b32(tag, rsx, WS_EPOCH, 0, AUX_SC1);
        if (live) STAMP(11);
      }
    }
  }
  DUMP();
}

extern "C" void kernel_launch(void* const* d_in, const int* in_sizes, int n_in, void* d_out, int out_size, void* d_ws, size_t ws_size, hipStream_t stream) {
  Params p;
  p.x = (const u16*)d_in[0]; p.kc = (const u16*)d_in[1]; p.vc = (const u16*)d_in[2]; p.wq = (const u16*)d_in[3]; p.wo = (const u16*)d_in[4];
  p.wg = (const u16*)d_in[6]; p.wu = (const u16*)d_in[7]; p.wd = (const u16*)d_in[8];
  p.out = (u16*)d_out; p.ws = (unsigned char*)d_ws;
  (void)hipFuncSetAttribute((const void*)fused_decode, hipFuncAttributeMaxDynamicSharedMemorySize, L_BYTES);
  hipLaunchKernelGGL(fused_decode, dim3(NWG), dim3(NTHR), L_BYTES, stream, p);
}
```

```cpp
#define PROBE 0
#include <hip/hip_runtime.h>
#include <stdint.h>

#define STAMPS 0
#define PACE "8"
#define DRYRUN 0
#define NOHELP 0
#define CONTIG 0
#define WAUX AUX_NT
#define QDELAY 450
#define EARLYW 1
#define SPEC 3
#define NOQWAIT 0
#define WIN 6
#define WCNT 44
#define BAL 1

typedef unsigned short u16;
typedef unsigned long long u64;
typedef unsigned u32x4 __attribute__((ext_vector_type(4)));
typedef unsigned u32x2 __attribute__((ext_vector_type(2)));
typedef float f32x4 __attribute__((ext_vector_type(4)));
typedef __bf16 bf16x8_t __attribute__((ext_vector_type(8)));
typedef __bf16 bf16x2_t __attribute__((ext_vector_type(2)));
typedef float f32x2 __attribute__((ext_vector_type(2)));
typedef short s16x4 __attribute__((ext_vector_type(4)));
#define GAS __attribute__((address_space(1)))
#define LAS __attribute__((address_space(3)))

constexpr int DM = 512, SK = 32768, FF = 1792;
constexpr int NWG = 256, NTHR = 512;
constexpr int AUX_NT = 2, AUX_SC1 = 16;
constexpr int RSRC3 = 0x00020000;
constexpr unsigned SPIN_CAP = 1u << 18;

constexpr int WS_EPOCH = 0;
constexpr int WS_Q = 4096;
constexpr int WS_HDR = WS_Q + 32768;
constexpr int WS_REC = WS_HDR + 8192;
constexpr int WS_HP = WS_REC + 524288;
constexpr int WS_DP = WS_HP + 65536;
constexpr int WS_OUTD = WS_DP + 56 * 8192;
constexpr int WS_SPAN = 2 << 20;
constexpr int WS_STAMP = 4 << 20;
static_assert(WS_OUTD + 4096 <= WS_SPAN, "ws map");

constexpr int L_SLAB = 0;
constexpr int L_Q = 98304;
constexpr int L_A = L_Q + 1024;
constexpr int L_ACT = L_A + 2176;
constexpr int L_MRG = L_ACT + 128;
constexpr int L_RED = L_MRG + 4 * 2560;
constexpr int L_CNT = L_RED + 64;
constexpr int L_BYTES = L_CNT + 64;
constexpr int C_QRDY = 0, C_TAG = 1, C_MRG = 2, C_HS = 3, C_GO = 4;
constexpr int ASTR = 1088;

struct Params {
  const u16 *x, *kc, *vc, *wq, *wo, *wg, *wu, *wd;
  u16* out;
  unsigned char* ws;
};

__device__ __forceinline__ float bf_lo(unsigned u) { return __builtin_bit_cast(float, u << 16); }
__device__ __forceinline__ float bf_hi(unsigned u) { return __builtin_bit_cast(float, u & 0xffff0000u); }
__device__ __forceinline__ unsigned pk_bf(float lo, float hi) {
  bf16x2_t r = __builtin_convertvector(f32x2{lo, hi}, bf16x2_t);
  return __builtin_bit_cast(unsigned, r);
}
__device__ __forceinline__ float rbf(float f) { return bf_lo(pk_bf(f, f)); }
template <int CTRL> __device__ __forceinline__ float dpp(float x) {
  return __builtin_bit_cast(float, __builtin_amdgcn_mov_dpp(__builtin_bit_cast(int, x), CTRL, 0xf, 0xf, true));
}
constexpr int XOR1 = 0xB1, XOR2 = 0x4E, HMIR = 0x141, MIR = 0x140, ROR8 = 0x128;
__device__ __forceinline__ float x16_32_max(float x) {
  auto s = __builtin_amdgcn_permlane16_swap(__float_as_uint(x), __float_as_uint(x), false, false);
  x = fmaxf(__uint_as_float(s[0]), __uint_as_float(s[1]));
  auto t = __builtin_amdgcn_permlane32_swap(__float_as_uint(x), __float_as_uint(x), false, false);
  return fmaxf(__uint_as_float(t[0]), __uint_as_float(t[1]));
}
__device__ __forceinline__ float x16_32_sum(float x) {
  auto s = __builtin_amdgcn_permlane16_swap(__float_as_uint(x), __float_as_uint(x), false, false);
  x = __uint_as_float(s[0]) + __uint_as_float(s[1]);
  auto t = __builtin_amdgcn_permlane32_swap(__float_as_uint(x), __float_as_uint(x), false, false);
  return __uint_as_float(t[0]) + __uint_as_float(t[1]);
}
__device__ __forceinline__ float row_sum(float v) { v += dpp<XOR1>(v); v += dpp<XOR2>(v); v += dpp<HMIR>(v); v += dpp<MIR>(v); return v; }
__device__ __forceinline__ float row_max(float v) { v = fmaxf(v, dpp<XOR1>(v)); v = fmaxf(v, dpp<XOR2>(v)); v = fmaxf(v, dpp<HMIR>(v)); v = fmaxf(v, dpp<MIR>(v)); return v; }
__device__ __forceinline__ float wave_sum(float v) { return x16_32_sum(row_sum(v)); }
__device__ __forceinline__ f32x4 mfma32(u32x4 a, u32x4 b, f32x4 c) {
  return __builtin_amdgcn_mfma_f32_16x16x32_bf16(__builtin_bit_cast(bf16x8_t, a), __builtin_bit_cast(bf16x8_t, b), c, 0, 0, 0);
}
__device__ __forceinline__ f32x4 mfma16(u32x2 a, u32x2 b, f32x4 c) {
  return __builtin_amdgcn_mfma_f32_16x16x16bf16_1k(__builtin_bit_cast(s16x4, a), __builtin_bit_cast(s16x4, b), c, 0, 0, 0);
}
#define MEMBAR() asm volatile("" ::: "memory")
#define WAIT_LDS() asm volatile("s_waitcnt lgkmcnt(0)" ::: "memory")
#define WAIT_VM() asm volatile("s_waitcnt vmcnt(0)" ::: "memory")

#define LD4X(C, d, off, rs, st) asm volatile("buffer_load_dwordx4 %0, %4, %5, 0 offen nt\n\tbuffer_load_dwordx4 %1, %4, %5, 0 offen offset:" #st " nt\n\t" \
    "buffer_load_dwordx4 %2, %4, %5, 0 offen offset:2*" #st " nt\n\tbuffer_load_dwordx4 %3, %4, %5, 0 offen offset:3*" #st " nt" \
    : C(d[0]), C(d[1]), C(d[2]), C(d[3]) : "v"(off), "s"(rs))
#define LD4(d, off, rs, st) LD4X("+v", d, off, rs, st)
#define LD4I(d, off, rs, st) LD4X("=&v", d, off, rs, st)
#define WAIT4(d, n) asm volatile("s_waitcnt vmcnt(" #n ")" : "+v"(d[0]), "+v"(d[1]), "+v"(d[2]), "+v"(d[3]))
#define WAIT4X(d, n) WAIT4(d, n)
__device__ __forceinline__ u32x4 make_srd(const void* base, unsigned bytes) {
  const u64 a = (u64)base;
  return u32x4{(unsigned)a, (unsigned)(a >> 32) & 0xffffu, bytes, (unsigned)RSRC3};
}
#define POLL2(d, off, rs, so) asm volatile("buffer_load_dwordx2 %0, %1, %2, %3 offen sc1" : "+v"(d) : "v"(off), "s"(rs), "s"(so) : "memory")
#define POLL4(d, off, rs, so) asm volatile("buffer_load_dwordx4 %0, %1, %2, %3 offen sc1" : "+v"(d) : "v"(off), "s"(rs), "s"(so) : "memory")
#define PIN4(a, b, c, d) asm volatile("" : "+v"(a), "+v"(b), "+v"(c), "+v"(d))

#define STAMP(i) do { } while (0)
#define STAMPC(i) do { } while (0)
#define DUMP() do { } while (0)

#define DMA16(src, dst) __builtin_amdgcn_global_load_lds((const GAS unsigned*)(src), (LAS unsigned*)(dst), 16, 0, WAUX)
__device__ __forceinline__ u32x4 tr_frag(LAS unsigned char* a, int plus4) {
  const u32x2 lo = __builtin_bit_cast(u32x2, __builtin_amdgcn_ds_read_tr16_b64_v4i16((LAS s16x4*)a));
  const u32x2 hi = __builtin_bit_cast(u32x2, __builtin_amdgcn_ds_read_tr16_b64_v4i16((LAS s16x4*)(a + plus4)));
  return u32x4{lo[0], lo[1], hi[0], hi[1]};
}
__device__ __forceinline__ f32x4 gemv16(LAS unsigned char* avec, int lane, const u32x4 (&bf)[16]) {
  LAS unsigned char* a = avec + (lane & 1) * ASTR + (lane >> 4) * 16;
  u32x4 af[16];
#pragma unroll
  for (int s = 0; s < 16; ++s) af[s] = *(LAS u32x4*)(a + s * 64);
  f32x4 d0 = {0.f, 0.f, 0.f, 0.f}, d1 = {0.f, 0.f, 0.f, 0.f};
#pragma unroll
  for (int s = 0; s < 16; s += 2) { d0 = mfma32(af[s], bf[s], d0); d1 = mfma32(af[s + 1], bf[s + 1], d1); }
  return d0 + d1;
}
__device__ __forceinline__ void lds_wait_ge(volatile LAS unsigned* w, unsigned v) {
#pragma unroll 1
  for (unsigned sp = 0; sp < (SPIN_CAP << 4); ++sp) { if ((unsigned)__builtin_amdgcn_readfirstlane(*w) >= v) break; __builtin_amdgcn_s_sleep(1); }
  MEMBAR();
}
#define LIVE_BARRIER() do { WAIT_LDS(); if (live) __builtin_amdgcn_s_barrier(); MEMBAR(); } while (0)

__global__ void __launch_bounds__(NTHR, 2) fused_decode(Params p) {
  u64 t_start; asm volatile("s_memrealtime %0\n\ts_waitcnt lgkmcnt(0)" : "=s"(t_start) :: "memory");
  __builtin_amdgcn_sched_barrier(0);
  extern __shared__ __attribute__((aligned(16))) unsigned char smem[];
  LAS unsigned char* L = (LAS unsigned char*)smem;
  volatile LAS unsigned* ctl = (volatile LAS unsigned*)(L + L_CNT);
  const int g = blockIdx.x, tid = threadIdx.x, lane = tid & 63, w = __builtin_amdgcn_readfirstlane(tid >> 6);
  const int kvh = g & 7, bat = g >> 7, split = (g >> 3) & 15;
  const __amdgpu_buffer_rsrc_t rsw = __builtin_amdgcn_make_buffer_rsrc((void*)p.ws, 0, 2 * WS_SPAN, RSRC3);
  const float LOG2E = 1.4426950408889634f;

  if (w < 4) {
    if (w == 0) { STAMP(0); STAMPC(20); }
    const int r = lane >> 4, c = lane & 15;
    int u0, nu;
    if (BAL) {
      if (split < 8) { u0 = 127 * split; nu = 127; }
      else if (bat == 0) { if (split < 12) { u0 = 1016 + 123 * (split - 8); nu = 123; } else { u0 = 1508 + 135 * (split - 12); nu = 135; } }
      else { if (split < 11) { u0 = 1016 + 123 * (split - 8); nu = 123; } else if (split < 14) { u0 = 1385 + 133 * (split - 11); nu = 133; } else { u0 = 1784 + 132 * (split - 14); nu = 132; } }
    } else { u0 = 128 * split; nu = 128; }
    const size_t base = ((size_t)(bat * 8 + kvh) * SK + (size_t)u0 * 16) * 256;
    const u32x4 rsk = make_srd((const unsigned char*)p.kc + base, nu * 4096), rsv = make_srd((const unsigned char*)p.vc + base, nu * 4096);
    int rem = (nu - w + 3) >> 2;
    int koff = w * 4096 + c * 256 + r * 16, voff = w * 4096 + r * 1024 + c * 16;
    constexpr int W = WIN;
    u32x4 kb[W][4], vb[W][4];
    __builtin_amdgcn_s_barrier();
    __builtin_amdgcn_sched_barrier(0);
#pragma unroll
    for (int j = 0; j < W; ++j) {
      LD4I(kb[j], koff, rsk, 64);
      if (j > 0) asm volatile("s_waitcnt vmcnt(" PACE ")");
      LD4I(vb[j], voff, rsv, 256);
      if (j > 0) asm volatile("s_waitcnt vmcnt(" PACE ")");
      koff += 16384; voff += 16384;
    }
    __builtin_amdgcn_sched_barrier(0);
    lds_wait_ge(ctl + C_QRDY, 1u);
    u32x4 qf[4];
#pragma unroll
    for (int s = 0; s < 4; ++s) qf[s] = *(LAS u32x4*)(L + L_Q + (c >> 2) * 256 + s * 64 + r * 16);
    if (w == 0) STAMP(1);
    const unsigned mask0 = (c & 3) == 0 ? 0x0000ffffu : ((c & 3) == 1 ? 0xffff0000u : 0u);
    const unsigned mask1 = (c & 3) == 2 ? 0x0000ffffu : ((c & 3) == 3 ? 0xffff0000u : 0u);
    const float SCALE = 0.08838834764831845f;
    float mL = -1.0e38f, l = 0.f;
    f32x4 o1 = {0.f, 0.f, 0.f, 0.f}, o2 = {0.f, 0.f, 0.f, 0.f};
#pragma unroll 1
    for (;;) {
#pragma unroll
      for (int j = 0; j < W; ++j) {
        f32x4 S = {0.f, 0.f, 0.f, 0.f};
        WAIT4X(kb[j], WCNT);
#pragma unroll
        for (int s = 0; s < 4; ++s) S = mfma32(kb[j][s], qf[s], S);
        LD4(kb[j], koff, rsk, 64);
        koff += 16384;
        const unsigned a01 = pk_bf(S[0], S[1]), a23 = pk_bf(S[2], S[3]);
        const unsigned b01 = pk_bf(bf_lo(a01) * SCALE, bf_hi(a01) * SCALE), b23 = pk_bf(bf_lo(a23) * SCALE, bf_hi(a23) * SCALE);
        const float s0 = bf_lo(b01), s1 = bf_hi(b01), s2 = bf_lo(b23), s3 = bf_hi(b23);
        const float mx = x16_32_max(fmaxf(fmaxf(s0, s1), fmaxf(s2, s3)));
        const float mn = fmaxf(mL, mx * LOG2E);
        const float alpha = __builtin_amdgcn_exp2f(mL - mn);
        mL = mn;
        float p0 = __builtin_amdgcn_exp2f(fmaf(s0, LOG2E, -mn)), p1 = __builtin_amdgcn_exp2f(fmaf(s1, LOG2E, -mn));
        float p2 = __builtin_amdgcn_exp2f(fmaf(s2, LOG2E, -mn)), p3 = __builtin_amdgcn_exp2f(fmaf(s3, LOG2E, -mn));
        l = fmaf(l, alpha, (p0 + p1) + (p2 + p3));
        o1 *= alpha; o2 *= alpha;
        PIN4(p0, p1, p2, p3);
        WAIT4X(vb[j], WCNT);
        const float pp[4] = {p0, p1, p2, p3};
#pragma unroll
        for (int i = 0; i < 4; ++i) {
          const unsigned pb = pk_bf(pp[i], pp[i]);
          const u32x2 bp = {pb & mask0, pb & mask1};
          o1 = mfma16(u32x2{vb[j][i][0], vb[j][i][1]}, bp, o1);
          o2 = mfma16(u32x2{vb[j][i][2], vb[j][i][3]}, bp, o2);
        }
        if (--rem == 0) goto stream_done;
        LD4(vb[j], voff, rsv, 256);
        voff += 16384;
      }
    }
  stream_done:
    l = x16_32_sum(l);
    LAS float* mg = (LAS float*)(L + L_MRG) + lane;
#pragma unroll
    for (int i = 0; i < 4; ++i) { mg[w * 640 + i * 64] = o1[i]; mg[w * 640 + (4 + i) * 64] = o2[i]; }
    mg[w * 640 + 512] = mL; mg[w * 640 + 576] = l;
    WAIT_LDS();
    unsigned old = 0;
    if (lane == 0) old = __hip_atomic_fetch_add((LAS unsigned*)(L + L_CNT) + C_MRG, 1u, __ATOMIC_RELAXED, __HIP_MEMORY_SCOPE_WORKGROUP);
    old = __builtin_amdgcn_readfirstlane(old);
    MEMBAR();
    if (old == 3) {
      const unsigned tag = __builtin_amdgcn_readfirstlane(ctl[C_TAG]);
      float mw[4], M, Lt = 0.f, acc[8];
#pragma unroll
      for (int u = 0; u < 4; ++u) mw[u] = mg[u * 640 + 512];
      M = fmaxf(fmaxf(mw[0], mw[1]), fmaxf(mw[2], mw[3]));
#pragma unroll
      for (int i = 0; i < 8; ++i) acc[i] = 0.f;
#pragma unroll
      for (int u = 0; u < 4; ++u) {
        const float f = __builtin_amdgcn_exp2f(mw[u] - M);
        Lt = fmaf(f, mg[u * 640 + 576], Lt);
#pragma unroll
        for (int i = 0; i < 8; ++i) acc[i] = fmaf(f, mg[u * 640 + i * 64], acc[i]);
      }
      const float inv = __builtin_amdgcn_rcpf(Lt);
      const int rec = bat * 16 + split, h = c >> 2;
      const int ro = WS_REC + (kvh * 32 + rec) * 2048 + h * 512 + (r * 16 + (c & 3) * 4) * 8;
#pragma unroll
      for (int i = 0; i < 4; i += 2)
        __builtin_amdgcn_raw_buffer_store_b128(u32x4{pk_bf(acc[i] * inv, acc[4 + i] * inv), tag, pk_bf(acc[i + 1] * inv, acc[5 + i] * inv), tag}, rsw, ro + i * 8, 0, AUX_SC1);
      if (r == 0 && (c & 3) == 0)
        __builtin_amdgcn_raw_buffer_store_b64(u32x2{__float_as_uint(M + __builtin_amdgcn_logf(Lt)), tag}, rsw, WS_HDR + ((kvh * 4 + h) * 32 + rec) * 8, 0, AUX_SC1);
      STAMP(3); STAMPC(21);
    }
#pragma unroll
    for (int j = 0; j < W; ++j) { WAIT4(kb[j], 0); WAIT4(vb[j], 0); }
    DUMP();
    return;
  }

  __builtin_amdgcn_s_setprio(3);
  const int u = w - 4;
  const int sub = bat * 4 + (split & 3);
  const int job = (split >> 2) == 2 && sub == 7 ? 4 : (split >> 2);
  const int fj = 7 * kvh + sub;
  const int rho = sub * 8 + kvh;
  LAS unsigned char* slab = L + L_SLAB;
  u32x4 bf[16], bd[8];
  unsigned ep = 0;
  u32x4 xv[2];
  unsigned xr[2] = {0u, 0u};
  if (u == 0) ep = __builtin_amdgcn_raw_buffer_load_b32(rsw, WS_EPOCH, 0, AUX_SC1);
  auto stage = [&]() {
    if (job == 1) {
      const unsigned char* src = (const unsigned char*)p.wo + (size_t)(kvh * 512 + u * 128 + 4 * (lane >> 3)) * 1024 + sub * 128 + (lane & 7) * 16;
#pragma unroll
      for (int i = 0; i < 16; ++i) DMA16(src + (32 * (i >> 2) + (i & 3)) * 1024, slab + (u * 16 + i) * 1024);
    } else if (job == 2) {
#pragma unroll
      for (int b = 0; b < 2; ++b) xr[b] = *(const GAS unsigned*)((const GAS unsigned char*)p.x + b * 1024 + (u * 128 + 2 * lane) * 2);
      const unsigned char* src = (const unsigned char*)((lane & 4) ? p.wu : p.wg) + (size_t)(u * 128 + (lane >> 3)) * (FF * 2) + fj * 64 + (lane & 3) * 16;
#pragma unroll
      for (int i = 0; i < 16; ++i) DMA16(src + (size_t)i * 8 * FF * 2, slab + (u * 16 + i) * 1024);
      const unsigned char* srd = (const unsigned char*)p.wd + (size_t)(fj * 32 + u * 8) * 1024 + lane * 16;
#pragma unroll
      for (int i = 0; i < 8; ++i) DMA16(srd + i * 1024, slab + 65536 + (u * 8 + i) * 1024);
    } else if (job == 3) {
      xr[0] = *(const GAS u16*)((const GAS unsigned char*)p.x + (rho >> 5) * 1024 + ((rho & 31) * 16 + (lane & 15)) * 2);
    }
  };
  if (job == 0 && NOHELP != 1) {
    if (u == 0) {
#pragma unroll
      for (int b = 0; b < 2; ++b) xv[b] = *(const GAS u32x4*)((const GAS unsigned char*)p.x + b * 1024 + lane * 16);
    }
    const unsigned char* src = (const unsigned char*)p.wq + (size_t)(u * 128 + (lane >> 3)) * 8192 + (kvh * 512 + sub * 64) * 2;
    const int pc = (lane & 7) ^ ((lane >> 3) & 2);
#pragma unroll
    for (int i = 0; i < 16; ++i) DMA16(src + (size_t)i * 8 * 8192 + (pc ^ ((i & 1) << 2)) * 16, slab + (u * 16 + i) * 1024);
  }
  if (EARLYW && NOHELP != 1 && NOHELP != 2) stage();
  if (u == 0) { if (lane < 16) ctl[lane] = 0u; WAIT_LDS(); }
  __builtin_amdgcn_sched_barrier(0);
  __builtin_amdgcn_s_barrier();
  __builtin_amdgcn_sched_barrier(0);
  if (NOHELP == 1) { DUMP(); return; }
  LAS unsigned char* fa = slab + ((lane >> 4) * 8 + ((lane >> 2) & 3)) * 128;
  u64 t_probe = 0;
  unsigned tag = 0;
  if (u == 0) {
    tag = __builtin_amdgcn_readfirstlane(ep) + 1u;
    ctl[C_TAG] = tag;
  }
  if (job == 0) {
    if (u == 0) {
#pragma unroll
      for (int b = 0; b < 2; ++b) {
        float v[8];
#pragma unroll
        for (int i = 0; i < 4; ++i) { v[2 * i] = bf_lo(xv[b][i]); v[2 * i + 1] = bf_hi(xv[b][i]); }
        float s = 0.f;
#pragma unroll
        for (int i = 0; i < 8; ++i) s += v[i];
        const float mean = wave_sum(s) * (1.0f / DM);
        float q2 = 0.f;
#pragma unroll
        for (int i = 0; i < 8; ++i) q2 = fmaf(v[i] - mean, v[i] - mean, q2);
        const float var = wave_sum(q2) * (1.0f / DM);
        const float mb = rbf(mean), rd = __builtin_amdgcn_rcpf(rbf(__builtin_amdgcn_sqrtf(rbf(rbf(var) + 1e-5f))));
        u32x4 o;
#pragma unroll
        for (int i = 0; i < 4; ++i) o[i] = pk_bf(rbf(v[2 * i] - mb) * rd, rbf(v[2 * i + 1] - mb) * rd);
        *(LAS u32x4*)(L + L_A + b * ASTR + lane * 16) = o;
      }
    }
    WAIT_VM();
    WAIT_LDS();
    if (lane == 0) __hip_atomic_fetch_add((LAS unsigned*)(L + L_CNT) + C_HS, 1u, __ATOMIC_RELAXED, __HIP_MEMORY_SCOPE_WORKGROUP);
    lds_wait_ge(ctl + C_HS, 4u);
    if (u == 0) STAMP(12);
    tag = __builtin_amdgcn_readfirstlane(ctl[C_TAG]);
    const int sw = ((lane >> 3) & 3) ^ u;
#pragma unroll
    for (int s = 0; s < 16; ++s) bf[s] = tr_frag(fa + sw * 32 + (lane & 3) * 8 + s * 4096, 512);
    const f32x4 d = gemv16(L + L_A, lane, bf);
    const float v = (lane & 16) ? d[1] : d[0];
    const float nb = dpp<XOR1>(v);
    if (lane < 32 && !(lane & 1))
      __builtin_amdgcn_raw_buffer_store_b64(u32x2{pk_bf(v, nb), tag}, rsw, WS_Q + ((lane >> 4) * 2048 + kvh * 256 + sub * 32 + u * 8 + ((lane & 15) >> 1)) * 8, 0, AUX_SC1);
    if (u == 0) STAMP(4);
  }
  if (u == 0) {
    const int qo = WS_Q + (bat * 2048 + kvh * 256 + lane * 2) * 8;
    u32x4 gq[2];
#pragma unroll 1
    for (unsigned sp = 0; sp < 4096u && __builtin_amdgcn_s_memrealtime() - t_start < QDELAY; ++sp) __builtin_amdgcn_s_sleep(2);
#pragma unroll 1
    for (unsigned sp = 0; sp < SPIN_CAP; ++sp) {
      MEMBAR();
#pragma unroll
      for (int i = 0; i < 2; ++i) gq[i] = __builtin_amdgcn_raw_buffer_load_b128(rsw, qo + i * 1024, 0, AUX_SC1);
      const bool ok = gq[0][1] == tag && gq[0][3] == tag && gq[1][1] == tag && gq[1][3] == tag;
      if (__all(ok)) break;
      __builtin_amdgcn_s_sleep(8);
    }
#pragma unroll
    for (int i = 0; i < 2; ++i) *(LAS u32x2*)(L + L_Q + (i * 128 + lane * 2) * 4) = u32x2{gq[i][0], gq[i][2]};
    WAIT_LDS();
    ctl[C_QRDY] = 1u;
    if (PROBE == 2) t_probe = __builtin_amdgcn_s_memrealtime() - t_start;
    STAMP(5);
  }
  if (NOHELP == 2) { DUMP(); return; }
  if (job == 0 || job == 4 || (job == 3 && u != 0)) { DUMP(); return; }
  if (u != 0) {
    lds_wait_ge(ctl + C_QRDY, 1u);
    tag = __builtin_amdgcn_readfirstlane(ctl[C_TAG]);
  }
  if (!EARLYW) stage();

  if (job != 3) {
    WAIT_VM();
    if (lane == 0) __hip_atomic_fetch_add((LAS unsigned*)(L + L_CNT) + C_HS, 1u, __ATOMIC_RELAXED, __HIP_MEMORY_SCOPE_WORKGROUP);
    lds_wait_ge(ctl + C_HS, 4u);
    const int co = job == 1 ? u * 32 + (lane & 3) * 8 : (lane & 2) * 32 + u * 16 + (lane & 1) * 8;
#pragma unroll
    for (int s = 0; s < 16; ++s) bf[s] = tr_frag(fa + co + s * 4096, 512);
    LAS unsigned char* fd = slab + 65536 + ((lane >> 4) * 8 + ((lane >> 2) & 3)) * 1024 + u * 256 + (lane & 3) * 8;
#pragma unroll
    for (int t = 0; t < 8; ++t) bd[t] = tr_frag(fd + t * 32, 4096);
  }
  if (NOHELP == 3) { asm volatile("" :: "v"(bf[0]), "v"(bd[0])); DUMP(); return; }
#pragma unroll 1
  for (int pass = DRYRUN ? 0 : 1; pass < 2; ++pass) {
    int live = pass;
    asm volatile("" : "+s"(live));
    const u32x4 rsp = make_srd(p.ws, live ? WS_SPAN : 0);
    const __amdgpu_buffer_rsrc_t rsx = __builtin_amdgcn_make_buffer_rsrc((void*)p.ws, 0, live ? WS_SPAN : 0, RSRC3);
    const int ho = WS_HDR + ((kvh * 4 + u) * 32 + (lane & 31)) * 8;
    if (job == 1) {
      const int ro = WS_REC + kvh * 65536 + u * 512 + lane * 8;
      u32x2 hd = {0u, 0u}, gr[32];
#pragma unroll
      for (int s = 0; s < 32; ++s) gr[s] = u32x2{0u, 0u};
      unsigned pend = 0xffu, want = live ? 0u : 0xffu;
#pragma unroll 1
      for (unsigned sp = 0; sp < SPIN_CAP; ++sp) {
        POLL2(hd, ho, rsp, 0);
#pragma unroll
        for (int q = 0; q < 8; ++q)
          if (want & (1u << q)) {
#pragma unroll
            for (int s = 4 * q; s < 4 * q + 4; ++s) POLL2(gr[s], ro, rsp, s * 2048);
          }
        asm volatile("s_waitcnt vmcnt(0)" : "+v"(hd) :: "memory");
#pragma unroll
        for (int q = 0; q < 8; ++q)
          if (want & (1u << q)) {
            PIN4(gr[4 * q], gr[4 * q + 1], gr[4 * q + 2], gr[4 * q + 3]);
            if (__all(gr[4 * q][1] == tag && gr[4 * q + 1][1] == tag && gr[4 * q + 2][1] == tag && gr[4 * q + 3][1] == tag)) pend &= ~(1u << q);
          }
        unsigned hm = (unsigned)__ballot(hd[1] == tag);
        if ((pend == 0 && hm == 0xffffffffu) || !live) break;
        const int miss = 32 - __builtin_popcount(hm);
        if (miss <= SPEC) want = pend;
        else {
          hm &= hm >> 1; hm &= hm >> 2;
          unsigned rdy = 0;
#pragma unroll
          for (int q = 0; q < 8; ++q) rdy |= ((hm >> (4 * q)) & 1u) << q;
          want = pend & rdy;
          if (miss == 32) __builtin_amdgcn_s_sleep(32); else __builtin_amdgcn_s_sleep(4);
        }
      }
      if (u == 0 && live) STAMP(6);
      const float lse = __uint_as_float(hd[0]);
      const float e = __builtin_amdgcn_exp2f(lse - row_max(lse));
      const float wn = e * __builtin_amdgcn_rcpf(row_sum(e));
      float a[4] = {0.f, 0.f, 0.f, 0.f};
#pragma unroll
      for (int s = 0; s < 32; ++s) {
        const float ws = __builtin_bit_cast(float, __builtin_amdgcn_readlane(__builtin_bit_cast(int, wn), s));
        a[(s >> 4) * 2] = fmaf(ws, bf_lo(gr[s][0]), a[(s >> 4) * 2]);
        a[(s >> 4) * 2 + 1] = fmaf(ws, bf_hi(gr[s][0]), a[(s >> 4) * 2 + 1]);
      }
      *(LAS unsigned*)(L + L_A + (u * 64 + lane) * 4) = pk_bf(a[0], a[1]);
      *(LAS unsigned*)(L + L_A + ASTR + (u * 64 + lane) * 4) = pk_bf(a[2], a[3]);
      LIVE_BARRIER();
      if (u == 0 && live) STAMP(13);
      const f32x4 d = gemv16(L + L_A, lane, bf);
      const float v = (lane & 16) ? d[1] : d[0];
      if (lane < 32)
        __builtin_amdgcn_raw_buffer_store_b64(u32x2{__float_as_uint(v), tag}, rsx, WS_HP + ((kvh * 2 + (lane >> 4)) * 512 + sub * 64 + u * 16 + (lane & 15)) * 8, 0, AUX_SC1);
      if (u == 0 && live) STAMP(7);
    } else {
      if (live) {
        if (u == 0) {
          lds_wait_ge(ctl + C_MRG, 4u);
          if (PROBE == 4) t_probe = __builtin_amdgcn_s_memrealtime() - t_start;
#pragma unroll 1
          for (unsigned sp = 0; sp < SPIN_CAP; ++sp) {
            MEMBAR();
            const u32x2 t = __builtin_amdgcn_raw_buffer_load_b64(rsx, ho, 0, AUX_SC1);
            if (__all(t[1] == tag)) break;
            __builtin_amdgcn_s_sleep(6);
          }
          ctl[C_GO] = 1u;
          if (PROBE == 1) t_probe = __builtin_amdgcn_s_memrealtime() - t_start;
        } else lds_wait_ge(ctl + C_GO, 1u);
      }
      if (job == 2) {
        const int po = WS_HP + (u * 128 + 2 * lane) * 8;
        u32x4 gp[16];
#pragma unroll
        for (int s = 0; s < 16; ++s) gp[s] = u32x4{0u, 0u, 0u, 0u};
        unsigned pend = 0xffu;
#pragma unroll 1
        for (unsigned sp = 0; sp < SPIN_CAP; ++sp) {
#pragma unroll
          for (int q = 0; q < 8; ++q)
            if (pend & (1u << q)) { POLL4(gp[2 * q], po, rsp, q * 8192); POLL4(gp[2 * q + 1], po, rsp, q * 8192 + 4096); }
          WAIT_VM();
#pragma unroll
          for (int q = 0; q < 8; ++q)
            if (pend & (1u << q)) {
              asm volatile("" : "+v"(gp[2 * q]), "+v"(gp[2 * q + 1]));
              if (__all(gp[2 * q][1] == tag && gp[2 * q][3] == tag && gp[2 * q + 1][1] == tag && gp[2 * q + 1][3] == tag)) pend &= ~(1u << q);
            }
          if (pend == 0 || !live) break;
        }
        if (u == 0 && live) STAMP(8);
        float h[2][2], mw[2], m2[2];
#pragma unroll
        for (int b = 0; b < 2; ++b) {
          float s0 = 0.f, s1 = 0.f;
#pragma unroll
          for (int k = 0; k < 8; ++k) { s0 += __uint_as_float(gp[2 * k + b][0]); s1 += __uint_as_float(gp[2 * k + b][2]); }
          h[b][0] = rbf(bf_lo(xr[b]) + rbf(s0)); h[b][1] = rbf(bf_hi(xr[b]) + rbf(s1));
          mw[b] = wave_sum(h[b][0] + h[b][1]) * (1.0f / 128);
          const float d0 = h[b][0] - mw[b], d1 = h[b][1] - mw[b];
          m2[b] = wave_sum(fmaf(d0, d0, d1 * d1));
        }
        if (lane == 0) *(LAS f32x4*)(L + L_RED + u * 16) = f32x4{mw[0], m2[0], mw[1], m2[1]};
        LIVE_BARRIER();
        if (u == 0 && live) STAMP(15);
        f32x4 st[4];
#pragma unroll
        for (int k = 0; k < 4; ++k) st[k] = *(LAS f32x4*)(L + L_RED + k * 16);
#pragma unroll
        for (int b = 0; b < 2; ++b) {
          const float mean = 0.25f * ((st[0][2 * b] + st[1][2 * b]) + (st[2][2 * b] + st[3][2 * b]));
          float ss = (st[0][2 * b + 1] + st[1][2 * b + 1]) + (st[2][2 * b + 1] + st[3][2 * b + 1]);
#pragma unroll
          for (int k = 0; k < 4; ++k) { const float dm = st[k][2 * b] - mean; ss = fmaf(128.0f * dm, dm, ss); }
          const float var = ss * (1.0f / DM);
          const float mb = rbf(mean), rd = __builtin_amdgcn_rcpf(rbf(__builtin_amdgcn_sqrtf(rbf(rbf(var) + 1e-5f))));
          *(LAS unsigned*)(L + L_A + b * ASTR + (u * 64 + lane) * 4) = pk_bf(rbf(h[b][0] - mb) * rd, rbf(h[b][1] - mb) * rd);
        }
        LIVE_BARRIER();
        if (u == 0 && live) STAMP(14);
        const f32x4 d = gemv16(L + L_A, lane, bf);
        const float gv = rbf((lane & 16) ? d[1] : d[0]);
        const float uv = dpp<ROR8>(gv);
        const float sg = rbf(gv * __builtin_amdgcn_rcpf(1.0f + __builtin_amdgcn_exp2f(-gv * LOG2E)));
        const float av = sg * uv;
        const float nb = dpp<XOR1>(av);
        if (lane < 32 && (lane & 9) == 0) *(LAS unsigned*)(L + L_ACT + (lane >> 4) * 64 + u * 16 + (lane & 7) * 2) = pk_bf(av, nb);
        LIVE_BARRIER();
        if (u == 0 && live) STAMP(16);
        const u32x4 aa = *(LAS u32x4*)(L + L_ACT + (lane & 1) * 64 + (lane >> 4) * 16);
        const int dpo = WS_DP + (fj * 1024 + ((lane >> 4) & 1) * 512 + u * 128 + (lane >> 5) * 16 + (lane & 15)) * 8;
#pragma unroll
        for (int t = 0; t < 8; t += 2) {
          const f32x4 e0 = mfma32(aa, bd[t], f32x4{0.f, 0.f, 0.f, 0.f}), e1 = mfma32(aa, bd[t + 1], f32x4{0.f, 0.f, 0.f, 0.f});
          const float v0 = (lane & 16) ? e0[1] : e0[0], v1 = (lane & 16) ? e1[1] : e1[0];
          __builtin_amdgcn_raw_buffer_store_b64(u32x2{__float_as_uint((lane & 32) ? v1 : v0), tag}, rsx, dpo + t * 128, 0, AUX_SC1);
        }
        if (u == 0 && live) STAMP(9);
      } else {
        const int co = (rho >> 5) * 512 + (rho & 31) * 16 + (lane & 15);
        u32x2 gp[8];
#pragma unroll 1
        for (unsigned sp = 0; sp < SPIN_CAP; ++sp) {
          MEMBAR();
#pragma unroll
          for (int s = 0; s < 8; ++s) gp[s] = __builtin_amdgcn_raw_buffer_load_b64(rsx, WS_HP + co * 8 + s * 8192, 0, AUX_SC1);
          bool ok = true;
#pragma unroll
          for (int s = 0; s < 8; ++s) ok = ok && gp[s][1] == tag;
          if (__all(ok) || !live) break;
          __builtin_amdgcn_s_sleep(4);
        }
        if (PROBE == 3) t_probe = __builtin_amdgcn_s_memrealtime() - t_start;
        float s0 = 0.f;
#pragma unroll
        for (int s = 0; s < 8; ++s) s0 += __uint_as_float(gp[s][0]);
        const float hres = rbf(bf_lo(xr[0]) + rbf(s0));
        u32x2 ga[14];
        const int ao = WS_DP + ((lane >> 4) * 1024 + co) * 8;
#pragma unroll 1
        for (unsigned sp = 0; sp < SPIN_CAP; ++sp) {
          MEMBAR();
#pragma unroll
          for (int i = 0; i < 14; ++i) ga[i] = __builtin_amdgcn_raw_buffer_load_b64(rsx, ao + i * 32768, 0, AUX_SC1);
          bool ok = true;
#pragma unroll
          for (int i = 0; i < 14; ++i) ok = ok && ga[i][1] == tag;
          if (__all(ok) || !live) break;
        }
        if (live) STAMP(10);
        float y = 0.f;
#pragma unroll
        for (int i = 0; i < 14; ++i) y += __uint_as_float(ga[i][0]);
        y = x16_32_sum(y);
        const unsigned ob = pk_bf(hres + rbf(y), 0.f);
        const __amdgpu_buffer_rsrc_t rso = __builtin_amdgcn_make_buffer_rsrc((void*)p.out, 0, live ? 2048 : 0, RSRC3);
        if (lane < 16) __builtin_amdgcn_raw_buffer_store_b16((u16)ob, rso, co * 2, 0, 0);
        if (rho == 0 && lane == 0) __builtin_amdgcn_raw_buffer_store_b32(tag, rsx, WS_EPOCH, 0, AUX_SC1);
        if (PROBE == 5) t_probe = __builtin_amdgcn_s_memrealtime() - t_start;
        if (rho == 0 || PROBE == 1 || PROBE == 2) { while (__builtin_amdgcn_s_memrealtime() - t_start < 8000 + t_probe) __builtin_amdgcn_s_sleep(1); }
      }
    }
  }
  DUMP();
}

extern "C" void kernel_launch(void* const* d_in, const int* in_sizes, int n_in, void* d_out, int out_size, void* d_ws, size_t ws_size, hipStream_t stream) {
  Params p;
  p.x = (const u16*)d_in[0]; p.kc = (const u16*)d_in[1]; p.vc = (const u16*)d_in[2]; p.wq = (const u16*)d_in[3]; p.wo = (const u16*)d_in[4];
  p.wg = (const u16*)d_in[6]; p.wu = (const u16*)d_in[7]; p.wd = (const u16*)d_in[8];
  p.out = (u16*)d_out; p.ws = (unsigned char*)d_ws;
  (void)hipFuncSetAttribute((const void*)fused_decode, hipFuncAttributeMaxDynamicSharedMemorySize, L_BYTES);
  hipLaunchKernelGGL(fused_decode, dim3(NWG), dim3(NTHR), L_BYTES, stream, p);
}
```

```cpp
#include <hip/hip_runtime.h>
#include <stdint.h>

#define STAMPS 0
#define PACE "8"
#define QDELAY 350
#define SPEC 3
#define WIN 4
#define WCNT 28
#define XW 2
#define OWNSPEC 1
#define DRY 0
#define TRICKLE 0
#define TUNE 1

typedef unsigned short u16;
typedef unsigned long long u64;
typedef unsigned u32x4 __attribute__((ext_vector_type(4)));
typedef unsigned u32x2 __attribute__((ext_vector_type(2)));
typedef float f32x4 __attribute__((ext_vector_type(4)));
typedef __bf16 bf16x8_t __attribute__((ext_vector_type(8)));
typedef __bf16 bf16x2_t __attribute__((ext_vector_type(2)));
typedef float f32x2 __attribute__((ext_vector_type(2)));
typedef short s16x4 __attribute__((ext_vector_type(4)));
#define GAS __attribute__((address_space(1)))
#define LAS __attribute__((address_space(3)))

constexpr int DM = 512, SK = 32768, FF = 1792;
constexpr int NWG = 256, NTHR = 512;
constexpr int AUX_NT = 2, AUX_SC1 = 16;
constexpr int RSRC3 = 0x00020000;
constexpr unsigned SPIN_CAP = 1u << 18;

constexpr int WS_EPOCH = 0;
constexpr int WS_Q = 8192;
constexpr int WS_HDR = WS_Q + 32768;
constexpr int WS_REC = WS_HDR + 32768;
constexpr int WS_HP = WS_REC + 524288;
constexpr int WS_DP = WS_HP + 32768;
constexpr int WS_SPAN = 2 << 20;
constexpr int WS_STAMP = 4 << 20;
static_assert(WS_DP + 56 * 4096 <= WS_SPAN, "ws map");

constexpr int ASTR = 1056;
constexpr int L_SLAB = 0;
constexpr int L_Q = 98304;
constexpr int L_A = L_Q + 1024;
constexpr int L_ACT = L_A + 2 * ASTR;
constexpr int L_MRG = L_ACT + 128;
constexpr int L_RED = L_MRG + 4 * 2560;
constexpr int L_CNT = L_RED + 128;
constexpr int L_BYTES = L_CNT + 64;
constexpr int C_QRDY = 0, C_TAG = 1, C_MRG = 2, C_HS = 3, C_GO = 4, C_WIN = 5, C_PUB = 6;

struct Params {
  const u16 *x, *kc, *vc, *wq, *wo, *wg, *wu, *wd;
  u16* out;
  unsigned char* ws;
  unsigned share[NWG];
};

__device__ __forceinline__ float bf_lo(unsigned u) { return __builtin_bit_cast(float, u << 16); }
__device__ __forceinline__ float bf_hi(unsigned u) { return __builtin_bit_cast(float, u & 0xffff0000u); }
__device__ __forceinline__ unsigned pk_bf(float lo, float hi) {
  bf16x2_t r = __builtin_convertvector(f32x2{lo, hi}, bf16x2_t);
  return __builtin_bit_cast(unsigned, r);
}
__device__ __forceinline__ float rbf(float f) { return bf_lo(pk_bf(f, f)); }
template <int CTRL> __device__ __forceinline__ float dpp(float x) {
  return __builtin_bit_cast(float, __builtin_amdgcn_mov_dpp(__builtin_bit_cast(int, x), CTRL, 0xf, 0xf, true));
}
constexpr int XOR1 = 0xB1, XOR2 = 0x4E, HMIR = 0x141, MIR = 0x140, ROR8 = 0x128, ROR4 = 0x124;
__device__ __forceinline__ float x16_32_max(float x) {
  auto s = __builtin_amdgcn_permlane16_swap(__float_as_uint(x), __float_as_uint(x), false, false);
  x = fmaxf(__uint_as_float(s[0]), __uint_as_float(s[1]));
  auto t = __builtin_amdgcn_permlane32_swap(__float_as_uint(x), __float_as_uint(x), false, false);
  return fmaxf(__uint_as_float(t[0]), __uint_as_float(t[1]));
}
__device__ __forceinline__ float add16(float a, float b) {
  auto s = __builtin_amdgcn_permlane16_swap(__float_as_uint(a), __float_as_uint(b), false, false);
  return __uint_as_float(s[0]) + __uint_as_float(s[1]);
}
__device__ __forceinline__ float add32(float a, float b) {
  auto s = __builtin_amdgcn_permlane32_swap(__float_as_uint(a), __float_as_uint(b), false, false);
  return __uint_as_float(s[0]) + __uint_as_float(s[1]);
}
__device__ __forceinline__ float x16_32_sum(float x) { x = add16(x, x); return add32(x, x); }
__device__ __forceinline__ float row_sum(float v) { v += dpp<XOR1>(v); v += dpp<XOR2>(v); v += dpp<HMIR>(v); v += dpp<MIR>(v); return v; }
__device__ __forceinline__ float row_max(float v) { v = fmaxf(v, dpp<XOR1>(v)); v = fmaxf(v, dpp<XOR2>(v)); v = fmaxf(v, dpp<HMIR>(v)); v = fmaxf(v, dpp<MIR>(v)); return v; }
__device__ __forceinline__ float wave_sum(float v) { return x16_32_sum(row_sum(v)); }
__device__ __forceinline__ float rdlane(float v, int l) { return __builtin_bit_cast(float, __builtin_amdgcn_readlane(__builtin_bit_cast(int, v), l)); }
__device__ __forceinline__ f32x4 mfma32(u32x4 a, u32x4 b, f32x4 c) {
  return __builtin_amdgcn_mfma_f32_16x16x32_bf16(__builtin_bit_cast(bf16x8_t, a), __builtin_bit_cast(bf16x8_t, b), c, 0, 0, 0);
}
__device__ __forceinline__ f32x4 mfma16(u32x2 a, u32x2 b, f32x4 c) {
  return __builtin_amdgcn_mfma_f32_16x16x16bf16_1k(__builtin_bit_cast(s16x4, a), __builtin_bit_cast(s16x4, b), c, 0, 0, 0);
}
#define MEMBAR() asm volatile("" ::: "memory")
#define WAIT_LDS() asm volatile("s_waitcnt lgkmcnt(0)" ::: "memory")
#define WAIT_VM() asm volatile("s_waitcnt vmcnt(0)" ::: "memory")
#define LBARRIER() do { WAIT_LDS(); if (live) __builtin_amdgcn_s_barrier(); MEMBAR(); } while (0)
#define BARRIER() do { WAIT_LDS(); __builtin_amdgcn_s_barrier(); MEMBAR(); } while (0)

#define LD4X(C, d, off, rs, st) asm volatile("buffer_load_dwordx4 %0, %4, %5, 0 offen nt\n\tbuffer_load_dwordx4 %1, %4, %5, 0 offen offset:" #st " nt\n\t" \
    "buffer_load_dwordx4 %2, %4, %5, 0 offen offset:2*" #st " nt\n\tbuffer_load_dwordx4 %3, %4, %5, 0 offen offset:3*" #st " nt" \
    : C(d[0]), C(d[1]), C(d[2]), C(d[3]) : "v"(off), "s"(rs))
#define LD4(d, off, rs, st) LD4X("+v", d, off, rs, st)
#define LD4I(d, off, rs, st) LD4X("=&v", d, off, rs, st)
#define WAIT4(d, n) asm volatile("s_waitcnt vmcnt(" #n ")" : "+v"(d[0]), "+v"(d[1]), "+v"(d[2]), "+v"(d[3]))
#define WAIT4X(d, n) WAIT4(d, n)
__device__ __forceinline__ u32x4 make_srd(const void* base, unsigned bytes) {
  const u64 a = (u64)base;
  return u32x4{(unsigned)a, (unsigned)(a >> 32) & 0xffffu, bytes, (unsigned)RSRC3};
}
#define POLL2(d, off, rs, so) asm volatile("buffer_load_dwordx2 %0, %1, %2, %3 offen sc1" : "+v"(d) : "v"(off), "s"(rs), "s"(so) : "memory")
#define POLL4(d, off, rs, so) asm volatile("buffer_load_dwordx4 %0, %1, %2, %3 offen sc1" : "+v"(d) : "v"(off), "s"(rs), "s"(so) : "memory")
#define PIN4(a, b, c, d) asm volatile("" : "+v"(a), "+v"(b), "+v"(c), "+v"(d))

#define STAMP(i) do { } while (0)
#define DUMP() do { } while (0)

#define TRK(i) do { if (TRICKLE) { _Pragma("unroll 1") for (unsigned sp_ = 0; sp_ < 100000u && __builtin_amdgcn_s_memrealtime() - t_start < (u64)(1000 + ((i) * 4 + u) * TRICKLE); ++sp_) __builtin_amdgcn_s_sleep(2); } } while (0)
#define DMA16(src, dst) __builtin_amdgcn_global_load_lds((const GAS unsigned*)(src), (LAS unsigned*)(dst), 16, 0, AUX_NT)
__device__ __forceinline__ u32x4 tr_frag(LAS unsigned char* a, int plus4) {
  const u32x2 lo = __builtin_bit_cast(u32x2, __builtin_amdgcn_ds_read_tr16_b64_v4i16((LAS s16x4*)a));
  const u32x2 hi = __builtin_bit_cast(u32x2, __builtin_amdgcn_ds_read_tr16_b64_v4i16((LAS s16x4*)(a + plus4)));
  return u32x4{lo[0], lo[1], hi[0], hi[1]};
}
__device__ __forceinline__ float gemv16(LAS unsigned char* avec, int lane, const u32x4 (&bf)[16]) {
  LAS unsigned char* a = avec + (lane & 1) * ASTR + ((lane >> 1) & 7) * 64 + (lane >> 4) * 16;
  const u32x4 a0 = *(LAS u32x4*)a, a1 = *(LAS u32x4*)(a + 512);
  f32x4 d[8];
#pragma unroll
  for (int s = 0; s < 8; ++s) d[s] = mfma32(a0, bf[s], f32x4{0.f, 0.f, 0.f, 0.f});
#pragma unroll
  for (int s = 0; s < 8; ++s) d[s] = mfma32(a1, bf[8 + s], d[s]);
  float v[2];
#pragma unroll
  for (int b = 0; b < 2; ++b) {
    const float t0 = d[0][b] + d[1][2 + b], t1 = d[2][b] + d[3][2 + b], t2 = d[4][b] + d[5][2 + b], t3 = d[6][b] + d[7][2 + b];
    v[b] = (lane & 32) ? ((lane & 16) ? t3 : t2) : ((lane & 16) ? t1 : t0);
  }
  const float x = add16(v[0], v[1]);
  return add32(x, x);
}
__device__ __forceinline__ unsigned tagw(float v, unsigned tb) { return (__float_as_uint(v) & 0xffffff00u) | tb; }
__device__ __forceinline__ void lds_wait_ge(volatile LAS unsigned* w, unsigned v) {
#pragma unroll 1
  for (unsigned sp = 0; sp < (SPIN_CAP << 4); ++sp) { if ((unsigned)__builtin_amdgcn_readfirstlane(*w) >= v) break; __builtin_amdgcn_s_sleep(1); }
  MEMBAR();
}
__device__ __forceinline__ unsigned lds_ticket(unsigned addr) {
  unsigned r; u64 keep;
  asm volatile("s_mov_b64 %1, exec\n\ts_mov_b64 exec, 1\n\tds_add_rtn_u32 %0, %2, %3\n\ts_mov_b64 exec, %1\n\ts_waitcnt lgkmcnt(0)"
               : "=&v"(r), "=&s"(keep) : "v"(addr), "v"(1u) : "memory");
  return __builtin_amdgcn_readfirstlane(r);
}

struct Acc { float mL, l; f32x4 o1, o2; };
#define EXIT() do { DUMP(); __builtin_amdgcn_endpgm(); } while (0)

__global__ void __launch_bounds__(NTHR, 2) fused_decode(Params p) {
  extern __shared__ __attribute__((aligned(16))) unsigned char smem[];
  LAS unsigned char* L = (LAS unsigned char*)smem;
  volatile LAS unsigned* ctl = (volatile LAS unsigned*)(L + L_CNT);
  const unsigned ctl_addr = (unsigned)(size_t)(L + L_CNT);
  const int g = blockIdx.x, tid = threadIdx.x, lane = tid & 63, w = __builtin_amdgcn_readfirstlane(tid >> 6);
  const int grp = g >> 4, mem = g & 15;
  const int kvh = grp & 7, bat = grp >> 3;
  const int jb = (mem + grp) & 3, ji = mem >> 2;
  const bool hasq = jb == 0 || jb == 3;
  const int qt = ((bat * 2 + (jb == 3)) * 2 + (ji & 1)) * 2 + (ji >> 1);
  const float LOG2E = 1.4426950408889634f;
  if (w == 4) {
    unsigned keep;
    asm volatile("s_mov_b32 %0, m0\n\ts_mov_b32 m0, %1\n\ts_nop 0\n\tglobal_load_lds_dwordx4 %2, %3\n\ts_add_u32 m0, m0, 0x400\n\ts_nop 0\n\tglobal_load_lds_dwordx4 %4, %3\n\t"
                 "s_add_u32 m0, m0, 0x400\n\ts_nop 0\n\tglobal_load_lds_dword %5, %6 sc1\n\ts_mov_b32 m0, %0"
                 : "=&s"(keep) : "s"((unsigned)(size_t)(L + L_MRG)), "v"(lane * 16), "s"(p.x), "v"(lane * 16 + 1024), "v"(0), "s"(p.ws) : "memory", "scc");
  }

  if (__builtin_expect(w >= 4, 0)) {
  __builtin_amdgcn_s_setprio(3);
  const u64 t_start = __builtin_amdgcn_s_memrealtime();
  const int u = w - 4;
  const int sub = bat * 4 + ji;
  const int job = jb == 2 && (grp & 3) == 3 && (ji & 1) ? 4 : jb;
  const int fj = (grp * 2 + (ji & 1) - (grp >> 2)) * 2 + (ji >> 1);
  const int rho = sub * 8 + kvh;
  LAS unsigned char* slab = L + L_SLAB;
  if (u == 0) { if (lane < 16) ctl[lane] = 0u; WAIT_LDS(); }
  __builtin_amdgcn_s_barrier();
  if ((hasq && u > 1) || (job == 4 && u != 0)) EXIT();
  const __amdgpu_buffer_rsrc_t rsw = __builtin_amdgcn_make_buffer_rsrc((void*)p.ws, 0, WS_SPAN, RSRC3);
  u32x4 bf[16], bd[8];
  LAS unsigned char* fa = slab + ((lane >> 4) * 8 + ((lane >> 2) & 3)) * 128;
  unsigned tag = 0;
  if (u == 0) {
    WAIT_VM();
    const unsigned ep = *(LAS unsigned*)(L + L_MRG + 2048);
    const u32x4 xv[2] = {*(LAS u32x4*)(L + L_MRG + lane * 16), *(LAS u32x4*)(L + L_MRG + 1024 + lane * 16)};
    if (hasq) {
#pragma unroll
      for (int b = 0; b < 2; ++b) {
        float v[8];
#pragma unroll
        for (int i = 0; i < 4; ++i) { v[2 * i] = bf_lo(xv[b][i]); v[2 * i + 1] = bf_hi(xv[b][i]); }
        float s = 0.f;
#pragma unroll
        for (int i = 0; i < 8; ++i) s += v[i];
        const float mean = wave_sum(s) * (1.0f / DM);
        float q2 = 0.f;
#pragma unroll
        for (int i = 0; i < 8; ++i) q2 = fmaf(v[i] - mean, v[i] - mean, q2);
        const float var = wave_sum(q2) * (1.0f / DM);
        const float mb = rbf(mean), rd = __builtin_amdgcn_rcpf(rbf(__builtin_amdgcn_sqrtf(rbf(rbf(var) + 1e-5f))));
        u32x4 o;
#pragma unroll
        for (int i = 0; i < 4; ++i) o[i] = pk_bf(rbf(v[2 * i] - mb) * rd, rbf(v[2 * i + 1] - mb) * rd);
        *(LAS u32x4*)(L + L_A + b * ASTR + lane * 16) = o;
      }
    }
    tag = __builtin_amdgcn_readfirstlane(ep) + 1u;
    ctl[C_TAG] = tag;
    if (hasq) { WAIT_LDS(); (void)lds_ticket(ctl_addr + C_HS * 4); }
  }
  if (hasq) {
    lds_wait_ge(ctl + C_HS, 5u);
    if (u == 0) STAMP(12);
    tag = __builtin_amdgcn_readfirstlane(ctl[C_TAG]);
    LAS unsigned char* fq = slab + ((lane >> 4) * 8 + ((lane >> 2) & 3)) * 64 + (((lane >> 4) & 1) ^ u) * 32 + (lane & 3) * 8;
#pragma unroll
    for (int s = 0; s < 16; ++s) bf[s] = tr_frag(fq + s * 2048, 256);
    const float v = gemv16(L + L_A, lane, bf);
    const float nb = dpp<XOR1>(v);
    if (lane < 32 && !(lane & 1))
      __builtin_amdgcn_raw_buffer_store_b64(u32x2{pk_bf(v, nb), tag}, rsw, WS_Q + ((lane >> 4) * 2048 + kvh * 256 + qt * 16 + u * 8 + ((lane & 15) >> 1)) * 8, 0, AUX_SC1);
    if (u != 0) EXIT();
    STAMP(4);
  }
  if (u == 0) {
    const int qo = WS_Q + (bat * 2048 + kvh * 256 + lane * 2) * 8;
    u32x4 gq[2];
#pragma unroll 1
    for (unsigned sp = 0; sp < 4096u && __builtin_amdgcn_s_memrealtime() - t_start < QDELAY; ++sp) __builtin_amdgcn_s_sleep(2);
#pragma unroll 1
    for (unsigned sp = 0; sp < SPIN_CAP; ++sp) {
      MEMBAR();
#pragma unroll
      for (int i = 0; i < 2; ++i) gq[i] = __builtin_amdgcn_raw_buffer_load_b128(rsw, qo + i * 1024, 0, AUX_SC1);
      const bool ok = gq[0][1] == tag && gq[0][3] == tag && gq[1][1] == tag && gq[1][3] == tag;
      if (__all(ok)) break;
      __builtin_amdgcn_s_sleep(8);
    }
#pragma unroll
    for (int i = 0; i < 2; ++i) *(LAS u32x2*)(L + L_Q + (i * 128 + lane * 2) * 4) = u32x2{gq[i][0], gq[i][2]};
    WAIT_LDS();
    ctl[C_QRDY] = 1u;
    STAMP(5);
    if (job == 0 || job == 4) EXIT();
  } else lds_wait_ge(ctl + C_WIN, 1u);
  float cs = 0.f;
  u32x2 xr = {0u, 0u};
  const int ocol = (rho >> 3) * 64 + ((((lane >> 4) & 1) << 1) | (lane >> 5)) * 16 + (rho & 3) * 4 + (lane & 3);
  if (job == 1) {
    const unsigned char* src = (const unsigned char*)p.wo + (size_t)(kvh * 512 + u * 128 + 4 * ((lane >> 3) & 3) + (lane >> 5)) * 1024 + sub * 128 + (lane & 7) * 16;
#pragma unroll
    for (int i = 0; i < 16; ++i) { TRK(i); DMA16(src + (32 * ((i >> 1) & 3) + 16 * (i >> 3) + 2 * (i & 1)) * 1024, slab + (u * 16 + i) * 1024); }
  } else if (job == 2) {
    xr = *(const GAS u32x2*)((const GAS unsigned char*)p.x + ((lane >> 2) & 1) * 1024 + (u * 128 + (lane >> 3) * 16 + (lane & 3) * 4) * 2);
    const unsigned char* src = (const unsigned char*)((lane & 4) ? p.wu : p.wg) + (size_t)(u * 128 + (lane >> 3)) * (FF * 2) + fj * 64 + (lane & 3) * 16;
#pragma unroll
    for (int i = 0; i < 16; ++i) { TRK(i); DMA16(src + (size_t)i * 8 * FF * 2, slab + (u * 16 + i) * 1024); }
    const unsigned char* srd = (const unsigned char*)p.wd + (size_t)(fj * 32 + u * 8) * 1024 + lane * 16;
#pragma unroll
    for (int i = 0; i < 8; ++i) DMA16(srd + i * 1024, slab + 65536 + (u * 8 + i) * 1024);
  } else if (job == 3) {
    xr[0] = *(const GAS u16*)((const GAS unsigned char*)p.x + ((rho >> 2) & 1) * 1024 + ocol * 2);
  }
  if (job == 1 || job == 2) {
    WAIT_VM();
    (void)lds_ticket(ctl_addr + C_HS * 4);
    lds_wait_ge(ctl + C_HS, 4u);
    tag = __builtin_amdgcn_readfirstlane(ctl[C_TAG]);
    const int co = job == 1 ? u * 32 + (lane & 3) * 8 : (lane & 2) * 32 + u * 16 + (lane & 1) * 8;
#pragma unroll
    for (int s = 0; s < 16; ++s) bf[s] = tr_frag(fa + co + s * 4096, 512);
    if (job == 2) {
      LAS unsigned char* fd = slab + 65536 + ((lane >> 4) * 8 + ((lane >> 2) & 3)) * 1024 + u * 256 + (lane & 3) * 8;
#pragma unroll
      for (int t = 0; t < 8; ++t) bd[t] = tr_frag(fd + t * 32, 4096);
      f32x4 d = {0.f, 0.f, 0.f, 0.f};
#pragma unroll
      for (int s = 0; s < 16; ++s) d = mfma32(u32x4{0x3f803f80u, 0x3f803f80u, 0x3f803f80u, 0x3f803f80u}, bf[s], d);
      cs = d[0];
    }
  }
  const unsigned tb = tag & 0xffu;
  const int ho = WS_HDR + (kvh * 32 + (lane & 31)) * 128 + u * 8;
#pragma unroll 1
  for (int pass = DRY ? 0 : 1; pass < 2; ++pass) {
  int live = pass;
  if (DRY) asm volatile("" : "+s"(live));
  const __amdgpu_buffer_rsrc_t rsx = __builtin_amdgcn_make_buffer_rsrc((void*)p.ws, 0, live ? WS_SPAN : 0, RSRC3);
  const u32x4 rsp = make_srd(p.ws, live ? WS_SPAN : 0);
  if (job == 1) {
    const int ro = WS_REC + kvh * 65536 + u * 512 + lane * 8;
    u32x2 hd = {0u, 0u}, gr[32];
#pragma unroll
    for (int s = 0; s < 32; ++s) gr[s] = u32x2{0u, 0u};
    if (live) lds_wait_ge(ctl + C_PUB, 1u);
    unsigned pend = 0xffu, want = OWNSPEC ? 0xffu : 0u;
#pragma unroll 1
    for (unsigned sp = 0; sp < SPIN_CAP; ++sp) {
      POLL2(hd, ho, rsp, 0);
#pragma unroll
      for (int q = 0; q < 8; ++q)
        if (want & (1u << q)) {
#pragma unroll
          for (int s = 4 * q; s < 4 * q + 4; ++s) POLL2(gr[s], ro, rsp, s * 2048);
        }
      asm volatile("s_waitcnt vmcnt(0)" : "+v"(hd) :: "memory");
#pragma unroll
      for (int q = 0; q < 8; ++q)
        if (want & (1u << q)) {
          PIN4(gr[4 * q], gr[4 * q + 1], gr[4 * q + 2], gr[4 * q + 3]);
          unsigned bad = 0;
#pragma unroll
          for (int s = 4 * q; s < 4 * q + 4; ++s) { gr[s] ^= tb; bad |= gr[s][0] | gr[s][1]; }
          if (__all((bad & 0xffu) == 0u)) pend &= ~(1u << q);
        }
      unsigned hm = (unsigned)__ballot(hd[1] == tag);
      if ((pend == 0 && hm == 0xffffffffu) || !live) break;
      const int miss = 32 - __builtin_popcount(hm);
      if (miss <= SPEC || OWNSPEC) want = pend;
      else {
        hm &= hm >> 1; hm &= hm >> 2;
        unsigned rdy = 0;
#pragma unroll
        for (int q = 0; q < 8; ++q) rdy |= ((hm >> (4 * q)) & 1u) << q;
        want = pend & rdy;
      }
    }
    if (u == 0) STAMP(6);
    const float lse = __uint_as_float(hd[0]);
    const float e = __builtin_amdgcn_exp2f(lse - row_max(lse));
    const float inv = __builtin_amdgcn_rcpf(row_sum(e));
    f32x2 a[2] = {f32x2{0.f, 0.f}, f32x2{0.f, 0.f}};
#pragma unroll
    for (int s = 0; s < 32; ++s) {
      const float ws = rdlane(e, s);
      a[s >> 4] = __builtin_elementwise_fma(f32x2{ws, ws}, __builtin_bit_cast(f32x2, gr[s]), a[s >> 4]);
    }
    const float i0 = rdlane(inv, 0), i1 = rdlane(inv, 16);
    *(LAS unsigned*)(L + L_A + (u * 64 + lane) * 4) = pk_bf(a[0][0] * i0, a[0][1] * i0);
    *(LAS unsigned*)(L + L_A + ASTR + (u * 64 + lane) * 4) = pk_bf(a[1][0] * i1, a[1][1] * i1);
    LBARRIER();
    if (u == 0) STAMP(13);
    const float v = gemv16(L + L_A, lane, bf);
    if (lane < 32) __builtin_amdgcn_raw_buffer_store_b32(tagw(v, tb), rsx, WS_HP + (kvh * 32 + sub * 4 + u) * 128 + lane * 4, 0, AUX_SC1);
    if (u == 0) STAMP(7);
    continue;
  }
  if (!live) { }
  else if (u == 0) {
    lds_wait_ge(ctl + C_PUB, 1u);
#pragma unroll 1
    for (unsigned sp = 0; sp < SPIN_CAP; ++sp) {
      MEMBAR();
      const u32x2 t = __builtin_amdgcn_raw_buffer_load_b64(rsx, ho, 0, AUX_SC1);
      if (__all(t[1] == tag)) break;
      __builtin_amdgcn_s_sleep(6);
    }
    ctl[C_GO] = 1u;
  } else lds_wait_ge(ctl + C_GO, 1u);
  if (job == 2) {
    const int po = WS_HP + u * 1024 + lane * 16;
    u32x4 gp[8];
#pragma unroll
    for (int s = 0; s < 8; ++s) gp[s] = u32x4{0u, 0u, 0u, 0u};
    unsigned pend = 0xffu;
#pragma unroll 1
    for (unsigned sp = 0; sp < SPIN_CAP; ++sp) {
#pragma unroll
      for (int q = 0; q < 8; ++q)
        if (pend & (1u << q)) POLL4(gp[q], po, rsp, q * 4096);
      WAIT_VM();
#pragma unroll
      for (int q = 0; q < 8; ++q)
        if (pend & (1u << q)) {
          asm volatile("" : "+v"(gp[q]));
          gp[q] ^= tb;
          if (__all(((gp[q][0] | gp[q][1] | gp[q][2] | gp[q][3]) & 0xffu) == 0u)) pend &= ~(1u << q);
        }
      if (pend == 0 || !live) break;
    }
    if (u == 0) STAMP(8);
    {
      f32x4 at = __builtin_bit_cast(f32x4, gp[0]);
#pragma unroll
      for (int k = 1; k < 8; ++k) at += __builtin_bit_cast(f32x4, gp[k]);
      const unsigned a01 = pk_bf(at[0], at[1]), a23 = pk_bf(at[2], at[3]);
      const unsigned h01 = pk_bf(bf_lo(xr[0]) + bf_lo(a01), bf_hi(xr[0]) + bf_hi(a01)), h23 = pk_bf(bf_lo(xr[1]) + bf_lo(a23), bf_hi(xr[1]) + bf_hi(a23));
      *(LAS u32x2*)(L + L_A + ((lane >> 2) & 1) * ASTR + (u * 128 + (lane >> 3) * 16 + (lane & 3) * 4) * 2) = u32x2{h01, h23};
      const float pv = bf_lo((lane & 4) ? __builtin_amdgcn_readlane(h01, 4) : __builtin_amdgcn_readlane(h01, 0));
      const float d0 = bf_lo(h01) - pv, d1 = bf_hi(h01) - pv, d2 = bf_lo(h23) - pv, d3 = bf_hi(h23) - pv;
      float t = add32((d0 + d1) + (d2 + d3), fmaf(d0, d0, d1 * d1) + fmaf(d2, d2, d3 * d3));
      t = add16(t, t);
      t += dpp<XOR1>(t); t += dpp<XOR2>(t); t += dpp<ROR8>(t);
      if ((lane & 27) == 0) *(LAS float*)(L + L_RED + ((lane >> 2) & 1) * 64 + u * 16 + (lane >> 5) * 4) = t;
      if ((lane & 59) == 1) *(LAS float*)(L + L_RED + ((lane >> 2) & 1) * 64 + u * 16 + 8) = pv;
    }
    LBARRIER();
    if (u == 0) STAMP(14);
    f32x4 st[4];
#pragma unroll
    for (int k = 0; k < 4; ++k) st[k] = *(LAS f32x4*)(L + L_RED + ((lane >> 4) & 1) * 64 + k * 16);
    const float dv = gemv16(L + L_A, lane, bf);
    float mk[4], ss = 0.f;
#pragma unroll
    for (int k = 0; k < 4; ++k) { mk[k] = fmaf(st[k][0], 1.0f / 128, st[k][2]); ss += fmaf(st[k][0] * (-1.0f / 128), st[k][0], st[k][1]); }
    const float mean = 0.25f * ((mk[0] + mk[1]) + (mk[2] + mk[3]));
#pragma unroll
    for (int k = 0; k < 4; ++k) { const float dm = mk[k] - mean; ss = fmaf(128.0f * dm, dm, ss); }
    const float mb = rbf(mean), rd = __builtin_amdgcn_rcpf(rbf(__builtin_amdgcn_sqrtf(rbf(rbf(fmaxf(ss, 0.f) * (1.0f / DM)) + 1e-5f))));
    const float gv = rbf(fmaf(-mb, cs, dv) * rd);
    const float uv = dpp<ROR8>(gv);
    const float sg = rbf(gv * __builtin_amdgcn_rcpf(1.0f + __builtin_amdgcn_exp2f(-gv * LOG2E)));
    const float av = sg * uv;
    const float nb = dpp<XOR1>(av);
    if (lane < 32 && (lane & 9) == 0) *(LAS unsigned*)(L + L_ACT + (lane >> 4) * 64 + u * 16 + (lane & 7) * 2) = pk_bf(av, nb);
    LBARRIER();
    if (u == 0) STAMP(16);
    const u32x4 aa = *(LAS u32x4*)(L + L_ACT + ((lane >> 2) & 1) * 64 + (lane >> 4) * 16);
    u32x4 o;
#pragma unroll
    for (int i = 0; i < 4; ++i) {
      const float e0 = mfma32(aa, bd[i], f32x4{0.f, 0.f, 0.f, 0.f})[0], e1 = mfma32(aa, bd[4 + i], f32x4{0.f, 0.f, 0.f, 0.f})[0];
      o[i] = tagw((lane & 32) ? e1 : e0, tb);
    }
    __builtin_amdgcn_raw_buffer_store_b128(o, rsx, WS_DP + fj * 4096 + u * 1024 + lane * 16, 0, AUX_SC1);
    if (u == 0) STAMP(9);
  } else {
    const int hpo = WS_HP + (ocol >> 4) * 128 + ((rho >> 2) & 1) * 64 + (ocol & 15) * 4;
    unsigned gp[8];
#pragma unroll 1
    for (unsigned sp = 0; sp < SPIN_CAP; ++sp) {
      MEMBAR();
#pragma unroll
      for (int s = 0; s < 8; ++s) gp[s] = __builtin_amdgcn_raw_buffer_load_b32(rsx, hpo + s * 4096, 0, AUX_SC1) ^ tb;
      unsigned bad = 0;
#pragma unroll
      for (int s = 0; s < 8; ++s) bad |= gp[s];
      if (__all((bad & 0xffu) == 0u) || !live) break;
      __builtin_amdgcn_s_sleep(4);
    }
    float s0 = __uint_as_float(gp[0]);
#pragma unroll
    for (int s = 1; s < 8; ++s) s0 += __uint_as_float(gp[s]);
    const float hres = rbf(bf_lo(xr[0]) + rbf(s0));
    u32x4 ga[4];
    const int ao = WS_DP + (lane >> 2) * 4096 + rho * 64 + (lane & 3) * 16;
    const int ao3 = lane < 32 ? ao + 3 * 65536 : WS_SPAN;
    const unsigned tb3 = lane < 32 ? tb : 0u;
#pragma unroll 1
    for (unsigned sp = 0; sp < SPIN_CAP; ++sp) {
      MEMBAR();
#pragma unroll
      for (int i = 0; i < 3; ++i) ga[i] = __builtin_amdgcn_raw_buffer_load_b128(rsx, ao, i * 65536, AUX_SC1) ^ tb;
      ga[3] = __builtin_amdgcn_raw_buffer_load_b128(rsx, ao3, 0, AUX_SC1) ^ tb3;
      const u32x4 bad = ga[0] | ga[1] | ga[2] | ga[3];
      if (__all(((bad[0] | bad[1] | bad[2] | bad[3]) & 0xffu) == 0u) || !live) break;
    }
    STAMP(10);
    const f32x4 y4 = (__builtin_bit_cast(f32x4, ga[0]) + __builtin_bit_cast(f32x4, ga[1])) + (__builtin_bit_cast(f32x4, ga[2]) + __builtin_bit_cast(f32x4, ga[3]));
    float y = add16(add32(y4[0], y4[1]), add32(y4[2], y4[3]));
    y += dpp<ROR8>(y); y += dpp<ROR4>(y);
    const unsigned ob = pk_bf(hres + rbf(y), 0.f);
    const __amdgpu_buffer_rsrc_t rso = __builtin_amdgcn_make_buffer_rsrc((void*)p.out, 0, live ? 2048 : 0, RSRC3);
    if ((lane & 12) == 0) __builtin_amdgcn_raw_buffer_store_b16((u16)ob, rso, (((rho >> 2) & 1) * 512 + ocol) * 2, 0, AUX_SC1);
    if (rho == 0 && lane == 0) __builtin_amdgcn_raw_buffer_store_b32(tag, rsx, WS_EPOCH, 0, AUX_SC1);
    STAMP(11);
  }
  }
  EXIT();
  }

  if (w == 0) STAMP(0);
  const int r = lane >> 4, c = lane & 15;
  const unsigned sh = p.share[g];
  const int ns = sh & 0xffff;
  const size_t base = (size_t)grp * SK * 256 + (size_t)(sh >> 16) * 4096;
  const u32x4 rsk = make_srd((const unsigned char*)p.kc + base, ns * 4096), rsv = make_srd((const unsigned char*)p.vc + base, ns * 4096);
  int koff = w * 4096 + c * 256 + r * 16, voff = w * 4096 + r * 1024 + c * 16;
  constexpr int W = WIN;
  u32x4 kb[W + XW][4], vb[W + XW][4];
  if (hasq) {
    const u32x4 rsq = make_srd(p.wq, 512 * 8192);
    const int qo = (lane >> 2) * 8192 + (((lane & 3) ^ ((lane >> 4) & 2)) << 4);
    int so = w * 128 * 8192 + (kvh * 512 + qt * 32) * 2;
    unsigned dst = (unsigned)(size_t)(L + L_SLAB) + w * 8192;
#pragma unroll 1
    for (int i = 0; i < 8; ++i) {
      unsigned keep;
      asm volatile("s_mov_b32 %0, m0\n\ts_mov_b32 m0, %1\n\ts_nop 0\n\tbuffer_load_dwordx4 %2, %3, %4 offen nt lds\n\ts_mov_b32 m0, %0"
                   : "=&s"(keep) : "s"(dst), "v"(qo), "s"(rsq), "s"(so) : "memory");
      so += 16 * 8192; dst += 1024;
    }
  }
  __builtin_amdgcn_sched_barrier(0);
#pragma unroll
  for (int jj = 0; jj < W + XW; ++jj) {
    const int j = jj < XW ? W + jj : jj - XW;
    LD4I(kb[j], koff, rsk, 64);
    koff += 16384;
    if (jj > 0) asm volatile("s_waitcnt vmcnt(" PACE ")");
    LD4I(vb[j], voff, rsv, 256);
    voff += 16384;
    if (jj > 0) asm volatile("s_waitcnt vmcnt(" PACE ")");
    else {
      __builtin_amdgcn_s_barrier();
      if (hasq) { asm volatile("s_waitcnt vmcnt(8)" ::: "memory"); (void)lds_ticket(ctl_addr + C_HS * 4); }
    }
  }
  __builtin_amdgcn_sched_barrier(0);
  ctl[C_WIN] = 1u;
  lds_wait_ge(ctl + C_QRDY, 1u);
  u32x4 qf[4];
#pragma unroll
  for (int s = 0; s < 4; ++s) qf[s] = *(LAS u32x4*)(L + L_Q + (c >> 2) * 256 + s * 64 + r * 16);
  if (w == 0) STAMP(1);
  const unsigned mask0 = (c & 3) == 0 ? 0x0000ffffu : ((c & 3) == 1 ? 0xffff0000u : 0u);
  const unsigned mask1 = (c & 3) == 2 ? 0x0000ffffu : ((c & 3) == 3 ? 0xffff0000u : 0u);
  const float SCALE = 0.08838834764831845f;
  float mL = -1.0e38f, l = 0.f;
  f32x4 o1 = {0.f, 0.f, 0.f, 0.f}, o2 = {0.f, 0.f, 0.f, 0.f};
  float pp[4];
  auto soft = [&](f32x4 S) {
    const unsigned a01 = pk_bf(S[0], S[1]), a23 = pk_bf(S[2], S[3]);
    const unsigned b01 = pk_bf(bf_lo(a01) * SCALE, bf_hi(a01) * SCALE), b23 = pk_bf(bf_lo(a23) * SCALE, bf_hi(a23) * SCALE);
    const float s0 = bf_lo(b01), s1 = bf_hi(b01), s2 = bf_lo(b23), s3 = bf_hi(b23);
    const float mx = x16_32_max(fmaxf(fmaxf(s0, s1), fmaxf(s2, s3)));
    const float mn = fmaxf(mL, mx * LOG2E);
    const float alpha = __builtin_amdgcn_exp2f(mL - mn);
    mL = mn;
    pp[0] = __builtin_amdgcn_exp2f(fmaf(s0, LOG2E, -mn)); pp[1] = __builtin_amdgcn_exp2f(fmaf(s1, LOG2E, -mn));
    pp[2] = __builtin_amdgcn_exp2f(fmaf(s2, LOG2E, -mn)); pp[3] = __builtin_amdgcn_exp2f(fmaf(s3, LOG2E, -mn));
    l = fmaf(l, alpha, (pp[0] + pp[1]) + (pp[2] + pp[3]));
    o1 *= alpha; o2 *= alpha;
  };
  auto pv = [&](const u32x4 (&v)[4]) {
#pragma unroll
    for (int i = 0; i < 4; ++i) {
      const unsigned pb = pk_bf(pp[i], pp[i]);
      const u32x2 bp = {pb & mask0, pb & mask1};
      o1 = mfma16(u32x2{v[i][0], v[i][1]}, bp, o1);
      o2 = mfma16(u32x2{v[i][2], v[i][3]}, bp, o2);
    }
  };
  int left = ((ns - w + 3) >> 2) - XW;
  bool extra = XW > 0;
#pragma unroll 1
  for (;;) {
#pragma unroll
    for (int j = 0; j < W; ++j) {
      f32x4 S = {0.f, 0.f, 0.f, 0.f};
      WAIT4X(kb[j], WCNT);
#pragma unroll
      for (int s = 0; s < 4; ++s) S = mfma32(kb[j][s], qf[s], S);
      LD4(kb[j], koff, rsk, 64);
      koff += 16384;
      soft(S);
      PIN4(pp[0], pp[1], pp[2], pp[3]);
      WAIT4X(vb[j], WCNT);
      pv(vb[j]);
      if (--left == 0) goto stream_done;
      LD4(vb[j], voff, rsv, 256);
      voff += 16384;
    }
    if (extra) {
      extra = false;
#pragma unroll
      for (int j = W; j < W + XW; ++j) {
        PIN4(kb[j][0], kb[j][1], kb[j][2], kb[j][3]);
        PIN4(vb[j][0], vb[j][1], vb[j][2], vb[j][3]);
        f32x4 S = {0.f, 0.f, 0.f, 0.f};
#pragma unroll
        for (int s = 0; s < 4; ++s) S = mfma32(kb[j][s], qf[s], S);
        soft(S);
        pv(vb[j]);
      }
    }
  }
stream_done:
  STAMP(2);
  l = x16_32_sum(l);
  LAS float* mg = (LAS float*)(L + L_MRG) + lane;
#pragma unroll
  for (int i = 0; i < 4; ++i) { mg[w * 640 + i * 64] = o1[i]; mg[w * 640 + (4 + i) * 64] = o2[i]; }
  mg[w * 640 + 512] = mL; mg[w * 640 + 576] = l;
  WAIT_LDS();
  if (lds_ticket(ctl_addr + C_MRG * 4) == 3) {
    const unsigned tag = ctl[C_TAG], tb = tag & 0xffu;
    const __amdgpu_buffer_rsrc_t rsw = __builtin_amdgcn_make_buffer_rsrc((void*)p.ws, 0, WS_SPAN, RSRC3);
    float mv[4][10];
#pragma unroll
    for (int u = 0; u < 4; ++u)
#pragma unroll
      for (int i = 0; i < 10; ++i) mv[u][i] = mg[u * 640 + i * 64];
    __builtin_amdgcn_sched_barrier(0x2 | 0x4);
    const float M = fmaxf(fmaxf(mv[0][8], mv[1][8]), fmaxf(mv[2][8], mv[3][8]));
    float Lt = 0.f, acc[8];
#pragma unroll
    for (int i = 0; i < 8; ++i) acc[i] = 0.f;
#pragma unroll
    for (int u = 0; u < 4; ++u) {
      const float f = __builtin_amdgcn_exp2f(mv[u][8] - M);
      Lt = fmaf(f, mv[u][9], Lt);
#pragma unroll
      for (int i = 0; i < 8; ++i) acc[i] = fmaf(f, mv[u][i], acc[i]);
    }
    const float inv = __builtin_amdgcn_rcpf(Lt);
    const int rec = bat * 16 + mem, h = c >> 2;
    const int ro = WS_REC + (kvh * 32 + rec) * 2048 + h * 512 + (r * 4 + (c & 3)) * 16;
#pragma unroll
    for (int i = 0; i < 2; ++i)
      __builtin_amdgcn_raw_buffer_store_b128(u32x4{tagw(acc[2 * i] * inv, tb), tagw(acc[4 + 2 * i] * inv, tb), tagw(acc[2 * i + 1] * inv, tb), tagw(acc[5 + 2 * i] * inv, tb)}, rsw, ro + i * 256, 0, AUX_SC1);
    if (r == 0 && (c & 3) == 0)
      __builtin_amdgcn_raw_buffer_store_b64(u32x2{__float_as_uint(M + __builtin_amdgcn_logf(Lt)), tag}, rsw, WS_HDR + (kvh * 32 + rec) * 128 + h * 8, 0, AUX_SC1);
    MEMBAR();
    ctl[C_PUB] = 1u;
    STAMP(3);
  }
#pragma unroll
  for (int j = 0; j < W; ++j) { WAIT4(kb[j], 0); WAIT4(vb[j], 0); }
  EXIT();
}

extern "C" void kernel_launch(void* const* d_in, const int* in_sizes, int n_in, void* d_out, int out_size, void* d_ws, size_t ws_size, hipStream_t stream) {
  Params p;
  p.x = (const u16*)d_in[0]; p.kc = (const u16*)d_in[1]; p.vc = (const u16*)d_in[2]; p.wq = (const u16*)d_in[3]; p.wo = (const u16*)d_in[4];
  p.wg = (const u16*)d_in[6]; p.wu = (const u16*)d_in[7]; p.wd = (const u16*)d_in[8];
  p.out = (u16*)d_out; p.ws = (unsigned char*)d_ws;
  for (int grp = 0; grp < 16; ++grp) {
    int cost[16], sum = 0;
    for (int m = 0; m < 16; ++m) {
      const int jb = (m + grp) & 3, ji = m >> 2, bat = grp >> 3;
      int c;
      if (jb == 1) c = TUNE ? 11 + (bat == 0 && ji == 3 ? (TUNE == 1 ? 3 : 0) : 0) : 8;
      else if (jb == 2) c = ((grp & 3) == 3 && (ji & 1)) ? (TUNE ? 0 : 12) : (TUNE ? 14 + (((grp * 2 + (ji & 1) - (grp >> 2)) & 3) == 3 ? (TUNE == 1 ? 4 : 0) : 0) : 12);
      else c = 4 + (TUNE && jb == 3 && bat == 0 && (ji & 1) ? 3 : 0);
      if (TUNE) c += (m & 1) ? -1 : 1;
      cost[m] = c; sum += c;
    }
    const int tot = 2048 + sum, lvl = tot / 16;
    int rem = tot - lvl * 16, first = 0;
    for (int m = 0; m < 16; ++m) {
      int n = lvl - cost[m];
      if (rem > 0 && (((m * 7) & 15) < tot - lvl * 16)) { ++n; --rem; }
      p.share[grp * 16 + m] = (unsigned)first << 16 | (unsigned)n;
      first += n;
    }
  }
  (void)hipFuncSetAttribute((const void*)fused_decode, hipFuncAttributeMaxDynamicSharedMemorySize, L_BYTES);
  hipLaunchKernelGGL(fused_decode, dim3(NWG), dim3(NTHR), L_BYTES, stream, p);
}
```

```cpp
#include <hip/hip_runtime.h>
#include <stdint.h>

#define STAMPS 0
#define PACE 5
#define QDELAY 400
#define SPEC 3
#define XW 8
#define OWNSPEC 1
#define DRY 0
#define C1 8
#define C2 15
#define CQ 4
#define CS1 3
#define CS2 4
#define CSQ 3
#define PAR 1

typedef unsigned short u16;
typedef unsigned long long u64;
typedef unsigned u32x4 __attribute__((ext_vector_type(4)));
typedef unsigned u32x2 __attribute__((ext_vector_type(2)));
typedef float f32x4 __attribute__((ext_vector_type(4)));
typedef __bf16 bf16x8_t __attribute__((ext_vector_type(8)));
typedef __bf16 bf16x2_t __attribute__((ext_vector_type(2)));
typedef float f32x2 __attribute__((ext_vector_type(2)));
typedef short s16x4 __attribute__((ext_vector_type(4)));
#define GAS __attribute__((address_space(1)))
#define LAS __attribute__((address_space(3)))

constexpr int DM = 512, SK = 32768, FF = 1792;
constexpr int NWG = 256, NTHR = 512;
constexpr int AUX_NT = 2, AUX_SC1 = 16;
constexpr int RSRC3 = 0x00020000;
constexpr unsigned SPIN_CAP = 1u << 18;

constexpr int WS_EPOCH = 0;
constexpr int WS_Q = 8192;
constexpr int WS_HDR = WS_Q + 32768;
constexpr int WS_REC = WS_HDR + 32768;
constexpr int WS_HP = WS_REC + 524288;
constexpr int WS_DP = WS_HP + 32768;
constexpr int WS_SPAN = 2 << 20;
constexpr int WS_STAMP = 4 << 20;
static_assert(WS_DP + 56 * 4096 <= WS_SPAN, "ws map");

constexpr int ASTR = 1056;
constexpr int L_SLAB = 0;
constexpr int L_Q = 98304;
constexpr int L_A = L_Q + 1024;
constexpr int L_ACT = L_A + 2 * ASTR;
constexpr int L_MRG = L_ACT + 128;
constexpr int L_RED = L_MRG + 4 * 2560;
constexpr int L_CNT = L_RED + 128;
constexpr int L_SC = L_CNT + 64;
constexpr int L_BYTES = L_SC + 4 * 40 * 128;
constexpr int C_QRDY = 0, C_TAG = 1, C_MRG = 2, C_HS = 3, C_GO = 4, C_WIN = 5, C_PUB = 6;

struct Params {
  const u16 *x, *kc, *vc, *wq, *wo, *wg, *wu, *wd;
  u16* out;
  unsigned char* ws;
  unsigned share[NWG];
};

__device__ __forceinline__ float bf_lo(unsigned u) { return __builtin_bit_cast(float, u << 16); }
__device__ __forceinline__ float bf_hi(unsigned u) { return __builtin_bit_cast(float, u & 0xffff0000u); }
__device__ __forceinline__ unsigned pk_bf(float lo, float hi) {
  bf16x2_t r = __builtin_convertvector(f32x2{lo, hi}, bf16x2_t);
  return __builtin_bit_cast(unsigned, r);
}
__device__ __forceinline__ float rbf(float f) { return bf_lo(pk_bf(f, f)); }
template <int CTRL> __device__ __forceinline__ float dpp(float x) {
  return __builtin_bit_cast(float, __builtin_amdgcn_mov_dpp(__builtin_bit_cast(int, x), CTRL, 0xf, 0xf, true));
}
constexpr int XOR1 = 0xB1, XOR2 = 0x4E, HMIR = 0x141, MIR = 0x140, ROR8 = 0x128, ROR4 = 0x124;
__device__ __forceinline__ float x16_32_max(float x) {
  auto s = __builtin_amdgcn_permlane16_swap(__float_as_uint(x), __float_as_uint(x), false, false);
  x = fmaxf(__uint_as_float(s[0]), __uint_as_float(s[1]));
  auto t = __builtin_amdgcn_permlane32_swap(__float_as_uint(x), __float_as_uint(x), false, false);
  return fmaxf(__uint_as_float(t[0]), __uint_as_float(t[1]));
}
__device__ __forceinline__ float add16(float a, float b) {
  auto s = __builtin_amdgcn_permlane16_swap(__float_as_uint(a), __float_as_uint(b), false, false);
  return __uint_as_float(s[0]) + __uint_as_float(s[1]);
}
__device__ __forceinline__ float add32(float a, float b) {
  auto s = __builtin_amdgcn_permlane32_swap(__float_as_uint(a), __float_as_uint(b), false, false);
  return __uint_as_float(s[0]) + __uint_as_float(s[1]);
}
__device__ __forceinline__ float x16_32_sum(float x) { x = add16(x, x); return add32(x, x); }
__device__ __forceinline__ float row_sum(float v) { v += dpp<XOR1>(v); v += dpp<XOR2>(v); v += dpp<HMIR>(v); v += dpp<MIR>(v); return v; }
__device__ __forceinline__ float row_max(float v) { v = fmaxf(v, dpp<XOR1>(v)); v = fmaxf(v, dpp<XOR2>(v)); v = fmaxf(v, dpp<HMIR>(v)); v = fmaxf(v, dpp<MIR>(v)); return v; }
__device__ __forceinline__ float wave_sum(float v) { return x16_32_sum(row_sum(v)); }
__device__ __forceinline__ float rdlane(float v, int l) { return __builtin_bit_cast(float, __builtin_amdgcn_readlane(__builtin_bit_cast(int, v), l)); }
__device__ __forceinline__ f32x4 mfma32(u32x4 a, u32x4 b, f32x4 c) {
  return __builtin_amdgcn_mfma_f32_16x16x32_bf16(__builtin_bit_cast(bf16x8_t, a), __builtin_bit_cast(bf16x8_t, b), c, 0, 0, 0);
}
__device__ __forceinline__ f32x4 mfma16(u32x2 a, u32x2 b, f32x4 c) {
  return __builtin_amdgcn_mfma_f32_16x16x16bf16_1k(__builtin_bit_cast(s16x4, a), __builtin_bit_cast(s16x4, b), c, 0, 0, 0);
}
#define MEMBAR() asm volatile("" ::: "memory")
#define WAIT_LDS() asm volatile("s_waitcnt lgkmcnt(0)" ::: "memory")
#define WAIT_VM() asm volatile("s_waitcnt vmcnt(0)" ::: "memory")
#define LBARRIER() do { WAIT_LDS(); if (live) __builtin_amdgcn_s_barrier(); MEMBAR(); } while (0)
#define BARRIER() do { WAIT_LDS(); __builtin_amdgcn_s_barrier(); MEMBAR(); } while (0)

#define R4(M, a) M(a, 0) M(a, 1) M(a, 2) M(a, 3)
#define R8(M) M(0) M(1) M(2) M(3) M(4) M(5) M(6) M(7)
#define STR2(x) #x
#define STR(x) STR2(x)
#define LD1X(C, d, off, rs) asm volatile("buffer_load_dwordx4 %0, %1, %2, 0 offen nt" : C(d) : "v"(off), "s"(rs))
#define LD1(d, off, rs, i) LD1X("+v", d, (off) + (i) * 4096, rs)
#define LD1I(d, off, rs, i) LD1X("=&v", d, (off) + (i) * 4096, rs)
#define WAITP(d) asm volatile("s_waitcnt vmcnt(" STR(PACE) ")" : "+v"(d))
#define WAIT4(d, n) asm volatile("s_waitcnt vmcnt(" #n ")" : "+v"(d[0]), "+v"(d[1]), "+v"(d[2]), "+v"(d[3]))
static_assert(PACE <= 7 && XW == 8, "ring of eight loads; eight tiles ahead");
__device__ __forceinline__ u32x4 make_srd(const void* base, unsigned bytes) {
  const u64 a = (u64)base;
  return u32x4{(unsigned)a, (unsigned)(a >> 32) & 0xffffu, bytes, (unsigned)RSRC3};
}
#define POLL2(d, off, rs, so) asm volatile("buffer_load_dwordx2 %0, %1, %2, %3 offen sc1" : "+v"(d) : "v"(off), "s"(rs), "s"(so) : "memory")
#define POLL4(d, off, rs, so) asm volatile("buffer_load_dwordx4 %0, %1, %2, %3 offen sc1" : "+v"(d) : "v"(off), "s"(rs), "s"(so) : "memory")
#define PIN4(a, b, c, d) asm volatile("" : "+v"(a), "+v"(b), "+v"(c), "+v"(d))

#define STAMP(i) do { } while (0)
#define DUMP() do { } while (0)

#define DMA16(src, dst) __builtin_amdgcn_global_load_lds((const GAS unsigned*)(src), (LAS unsigned*)(dst), 16, 0, AUX_NT)
__device__ __forceinline__ u32x4 tr_frag(LAS unsigned char* a, int plus4) {
  const u32x2 lo = __builtin_bit_cast(u32x2, __builtin_amdgcn_ds_read_tr16_b64_v4i16((LAS s16x4*)a));
  const u32x2 hi = __builtin_bit_cast(u32x2, __builtin_amdgcn_ds_read_tr16_b64_v4i16((LAS s16x4*)(a + plus4)));
  return u32x4{lo[0], lo[1], hi[0], hi[1]};
}
__device__ __forceinline__ float gemv16(LAS unsigned char* avec, int lane, const u32x4 (&bf)[16]) {
  LAS unsigned char* a = avec + (lane & 1) * ASTR + ((lane >> 1) & 7) * 64 + (lane >> 4) * 16;
  const u32x4 a0 = *(LAS u32x4*)a, a1 = *(LAS u32x4*)(a + 512);
  f32x4 d[8];
#pragma unroll
  for (int s = 0; s < 8; ++s) d[s] = mfma32(a0, bf[s], f32x4{0.f, 0.f, 0.f, 0.f});
#pragma unroll
  for (int s = 0; s < 8; ++s) d[s] = mfma32(a1, bf[8 + s], d[s]);
  float v[2];
#pragma unroll
  for (int b = 0; b < 2; ++b) {
    const float t0 = d[0][b] + d[1][2 + b], t1 = d[2][b] + d[3][2 + b], t2 = d[4][b] + d[5][2 + b], t3 = d[6][b] + d[7][2 + b];
    v[b] = (lane & 32) ? ((lane & 16) ? t3 : t2) : ((lane & 16) ? t1 : t0);
  }
  const float x = add16(v[0], v[1]);
  return add32(x, x);
}
__device__ __forceinline__ unsigned tagw(float v, unsigned tb) { return (__float_as_uint(v) & 0xffffff00u) | tb; }
__device__ __forceinline__ void lds_wait_ge(volatile LAS unsigned* w, unsigned v) {
#pragma unroll 1
  for (unsigned sp = 0; sp < (SPIN_CAP << 4); ++sp) { if ((unsigned)__builtin_amdgcn_readfirstlane(*w) >= v) break; __builtin_amdgcn_s_sleep(1); }
  MEMBAR();
}
__device__ __forceinline__ unsigned lds_ticket(unsigned addr) {
  unsigned r; u64 keep;
  asm volatile("s_mov_b64 %1, exec\n\ts_mov_b64 exec, 1\n\tds_add_rtn_u32 %0, %2, %3\n\ts_mov_b64 exec, %1\n\ts_waitcnt lgkmcnt(0)"
               : "=&v"(r), "=&s"(keep) : "v"(addr), "v"(1u) : "memory");
  return __builtin_amdgcn_readfirstlane(r);
}

#define EXIT() do { DUMP(); __builtin_amdgcn_endpgm(); } while (0)

__global__ void __launch_bounds__(NTHR, 2) fused_decode(Params p) {
  extern __shared__ __attribute__((aligned(16))) unsigned char smem[];
  LAS unsigned char* L = (LAS unsigned char*)smem;
  volatile LAS unsigned* ctl = (volatile LAS unsigned*)(L + L_CNT);
  const unsigned ctl_addr = (unsigned)(size_t)(L + L_CNT);
  const int g = blockIdx.x, tid = threadIdx.x, lane = tid & 63, w = __builtin_amdgcn_readfirstlane(tid >> 6);
  const int grp = g >> 4, mem = g & 15;
  const int kvh = grp & 7, bat = grp >> 3;
  const int jb = (mem + grp) & 3, ji = mem >> 2;
  const bool hasq = jb == 0 || jb == 3;
  const int qt = ((bat * 2 + (jb == 3)) * 2 + (ji & 1)) * 2 + (ji >> 1);
  const float LOG2E = 1.4426950408889634f;
  if (w == 4) {
    unsigned keep;
    asm volatile("s_mov_b32 %0, m0\n\ts_mov_b32 m0, %1\n\ts_nop 0\n\tglobal_load_lds_dwordx4 %2, %3\n\ts_add_u32 m0, m0, 0x400\n\ts_nop 0\n\tglobal_load_lds_dwordx4 %4, %3\n\t"
                 "s_add_u32 m0, m0, 0x400\n\ts_nop 0\n\tglobal_load_lds_dword %5, %6 sc1\n\ts_mov_b32 m0, %0"
                 : "=&s"(keep) : "s"((unsigned)(size_t)(L + L_MRG)), "v"(lane * 16), "s"(p.x), "v"(lane * 16 + 1024), "v"(0), "s"(p.ws) : "memory", "scc");
  }

  if (__builtin_expect(w >= 4, 0)) {
  __builtin_amdgcn_s_setprio(3);
  const u64 t_start = __builtin_amdgcn_s_memrealtime();
  const int u = w - 4;
  const int sub = bat * 4 + ji;
  const int job = jb == 2 && (grp & 3) == 3 && (ji & 1) ? 4 : jb;
  const int fj = (grp * 2 + (ji & 1) - (grp >> 2)) * 2 + (ji >> 1);
  const int rho = sub * 8 + kvh;
  LAS unsigned char* slab = L + L_SLAB;
  if (u == 0) { if (lane < 16) ctl[lane] = 0u; WAIT_LDS(); }
  __builtin_amdgcn_s_barrier();
  if ((hasq && u > 1) || (job == 4 && u != 0)) EXIT();
  const __amdgpu_buffer_rsrc_t rsw = __builtin_amdgcn_make_buffer_rsrc((void*)p.ws, 0, WS_SPAN, RSRC3);
  u32x4 bf[16], bd[8];
  LAS unsigned char* fa = slab + ((lane >> 4) * 8 + ((lane >> 2) & 3)) * 128;
  unsigned tag = 0;
  if (u == 0) {
    WAIT_VM();
    const unsigned ep = *(LAS unsigned*)(L + L_MRG + 2048);
    const u32x4 xv[2] = {*(LAS u32x4*)(L + L_MRG + lane * 16), *(LAS u32x4*)(L + L_MRG + 1024 + lane * 16)};
    if (hasq) {
#pragma unroll
      for (int b = 0; b < 2; ++b) {
        float v[8];
#pragma unroll
        for (int i = 0; i < 4; ++i) { v[2 * i] = bf_lo(xv[b][i]); v[2 * i + 1] = bf_hi(xv[b][i]); }
        float s = 0.f;
#pragma unroll
        for (int i = 0; i < 8; ++i) s += v[i];
        const float mean = wave_sum(s) * (1.0f / DM);
        float q2 = 0.f;
#pragma unroll
        for (int i = 0; i < 8; ++i) q2 = fmaf(v[i] - mean, v[i] - mean, q2);
        const float var = wave_sum(q2) * (1.0f / DM);
        const float mb = rbf(mean), rd = __builtin_amdgcn_rcpf(rbf(__builtin_amdgcn_sqrtf(rbf(rbf(var) + 1e-5f))));
        u32x4 o;
#pragma unroll
        for (int i = 0; i < 4; ++i) o[i] = pk_bf(rbf(v[2 * i] - mb) * rd, rbf(v[2 * i + 1] - mb) * rd);
        *(LAS u32x4*)(L + L_A + b * ASTR + lane * 16) = o;
      }
    }
    tag = __builtin_amdgcn_readfirstlane(ep) + 1u;
    ctl[C_TAG] = tag;
    if (hasq) { WAIT_LDS(); (void)lds_ticket(ctl_addr + C_HS * 4); }
  }
  if (hasq) {
    lds_wait_ge(ctl + C_HS, 5u);
    if (u == 0) STAMP(12);
    tag = __builtin_amdgcn_readfirstlane(ctl[C_TAG]);
    LAS unsigned char* fq = slab + ((lane >> 4) * 8 + ((lane >> 2) & 3)) * 64 + (((lane >> 4) & 1) ^ u) * 32 + (lane & 3) * 8;
#pragma unroll
    for (int s = 0; s < 16; ++s) bf[s] = tr_frag(fq + s * 2048, 256);
    const float v = gemv16(L + L_A, lane, bf);
    const float nb = dpp<XOR1>(v);
    if (lane < 32 && !(lane & 1))
      __builtin_amdgcn_raw_buffer_store_b64(u32x2{pk_bf(v, nb), tag}, rsw, WS_Q + ((lane >> 4) * 2048 + kvh * 256 + qt * 16 + u * 8 + ((lane & 15) >> 1)) * 8, 0, AUX_SC1);
    if (u != 0) EXIT();
    STAMP(4);
  }
  if (u == 0) {
    const int qo = WS_Q + (bat * 2048 + kvh * 256 + lane * 2) * 8;
    u32x4 gq[2];
#pragma unroll 1
    for (unsigned sp = 0; sp < 4096u && __builtin_amdgcn_s_memrealtime() - t_start < QDELAY; ++sp) __builtin_amdgcn_s_sleep(2);
#pragma unroll 1
    for (unsigned sp = 0; sp < SPIN_CAP; ++sp) {
      MEMBAR();
#pragma unroll
      for (int i = 0; i < 2; ++i) gq[i] = __builtin_amdgcn_raw_buffer_load_b128(rsw, qo + i * 1024, 0, AUX_SC1);
      const bool ok = gq[0][1] == tag && gq[0][3] == tag && gq[1][1] == tag && gq[1][3] == tag;
      if (__all(ok)) break;
      __builtin_amdgcn_s_sleep(8);
    }
#pragma unroll
    for (int i = 0; i < 2; ++i) *(LAS u32x2*)(L + L_Q + (i * 128 + lane * 2) * 4) = u32x2{gq[i][0], gq[i][2]};
    WAIT_LDS();
    ctl[C_QRDY] = 1u;
    STAMP(5);
    if (job == 0 || job == 4) EXIT();
  } else lds_wait_ge(ctl + C_WIN, 1u);
  float cs = 0.f;
  u32x2 xr = {0u, 0u};
  const int ocol = (rho >> 3) * 64 + ((((lane >> 4) & 1) << 1) | (lane >> 5)) * 16 + (rho & 3) * 4 + (lane & 3);
  if (job == 1) {
    const unsigned char* src = (const unsigned char*)p.wo + (size_t)(kvh * 512 + u * 128 + 4 * ((lane >> 3) & 3) + (lane >> 5)) * 1024 + sub * 128 + (lane & 7) * 16;
#pragma unroll
    for (int i = 0; i < 16; ++i) { DMA16(src + (32 * ((i >> 1) & 3) + 16 * (i >> 3) + 2 * (i & 1)) * 1024, slab + (u * 16 + i) * 1024); }
  } else if (job == 2) {
    xr = *(const GAS u32x2*)((const GAS unsigned char*)p.x + ((lane >> 2) & 1) * 1024 + (u * 128 + (lane >> 3) * 16 + (lane & 3) * 4) * 2);
    const unsigned char* src = (const unsigned char*)((lane & 4) ? p.wu : p.wg) + (size_t)(u * 128 + (lane >> 3)) * (FF * 2) + fj * 64 + (lane & 3) * 16;
#pragma unroll
    for (int i = 0; i < 16; ++i) { DMA16(src + (size_t)i * 8 * FF * 2, slab + (u * 16 + i) * 1024); }
    const unsigned char* srd = (const unsigned char*)p.wd + (size_t)(fj * 32 + u * 8) * 1024 + lane * 16;
#pragma unroll
    for (int i = 0; i < 8; ++i) DMA16(srd + i * 1024, slab + 65536 + (u * 8 + i) * 1024);
  } else if (job == 3) {
    xr[0] = *(const GAS u16*)((const GAS unsigned char*)p.x + ((rho >> 2) & 1) * 1024 + ocol * 2);
  }
  if (job == 1 || job == 2) {
    WAIT_VM();
    (void)lds_ticket(ctl_addr + C_HS * 4);
    lds_wait_ge(ctl + C_HS, 4u);
    tag = __builtin_amdgcn_readfirstlane(ctl[C_TAG]);
    const int co = job == 1 ? u * 32 + (lane & 3) * 8 : (lane & 2) * 32 + u * 16 + (lane & 1) * 8;
#pragma unroll
    for (int s = 0; s < 16; ++s) bf[s] = tr_frag(fa + co + s * 4096, 512);
    if (job == 2) {
      LAS unsigned char* fd = slab + 65536 + ((lane >> 4) * 8 + ((lane >> 2) & 3)) * 1024 + u * 256 + (lane & 3) * 8;
#pragma unroll
      for (int t = 0; t < 8; ++t) bd[t] = tr_frag(fd + t * 32, 4096);
      f32x4 d = {0.f, 0.f, 0.f, 0.f};
#pragma unroll
      for (int s = 0; s < 16; ++s) d = mfma32(u32x4{0x3f803f80u, 0x3f803f80u, 0x3f803f80u, 0x3f803f80u}, bf[s], d);
      cs = d[0];
    }
  }
  const unsigned tb = tag & 0xffu;
  const int ho = WS_HDR + (kvh * 32 + (lane & 31)) * 128 + u * 8;
#pragma unroll 1
  for (int pass = DRY ? 0 : 1; pass < 2; ++pass) {
  int live = pass;
  if (DRY) asm volatile("" : "+s"(live));
  const __amdgpu_buffer_rsrc_t rsx = __builtin_amdgcn_make_buffer_rsrc((void*)p.ws, 0, live ? WS_SPAN : 0, RSRC3);
  const u32x4 rsp = make_srd(p.ws, live ? WS_SPAN : 0);
  if (job == 1) {
    const int ro = WS_REC + kvh * 65536 + u * 512 + lane * 8;
    u32x2 hd = {0u, 0u}, gr[32];
#define GZ1(q, i) gr[4 * q + i] = u32x2{0u, 0u};
#define GZ(q) R4(GZ1, q)
    R8(GZ)
    if (live) lds_wait_ge(ctl + C_PUB, 1u);
    unsigned pend = 0xffu, want = OWNSPEC ? 0xffu : 0u;
#pragma unroll 1
    for (unsigned sp = 0; sp < SPIN_CAP; ++sp) {
      POLL2(hd, ho, rsp, 0);
#define GL1(q, i) POLL2(gr[4 * q + i], ro, rsp, (4 * q + i) * 2048);
#define GL(q) if (want & (1u << q)) { R4(GL1, q) }
      R8(GL)
      asm volatile("s_waitcnt vmcnt(0)" : "+v"(hd) :: "memory");
#define GC1(q, i) gr[4 * q + i] ^= tb; bad |= gr[4 * q + i][0] | gr[4 * q + i][1];
#define GC(q) if (want & (1u << q)) { PIN4(gr[4 * q], gr[4 * q + 1], gr[4 * q + 2], gr[4 * q + 3]); unsigned bad = 0; R4(GC1, q) if (__all((bad & 0xffu) == 0u)) pend &= ~(1u << q); }
      R8(GC)
      unsigned hm = (unsigned)__ballot(hd[1] == tag);
      if ((pend == 0 && hm == 0xffffffffu) || !live) break;
      const int miss = 32 - __builtin_popcount(hm);
      if (miss <= SPEC || OWNSPEC) want = pend;
      else {
        hm &= hm >> 1; hm &= hm >> 2;
        unsigned rdy = 0;
#pragma unroll
        for (int q = 0; q < 8; ++q) rdy |= ((hm >> (4 * q)) & 1u) << q;
        want = pend & rdy;
      }
    }
    if (u == 0) STAMP(6);
    const float lse = __uint_as_float(hd[0]);
    const float e = __builtin_amdgcn_exp2f(lse - row_max(lse));
    const float inv = __builtin_amdgcn_rcpf(row_sum(e));
    f32x2 a[2] = {f32x2{0.f, 0.f}, f32x2{0.f, 0.f}};
#define GF1(q, i) { const float ws = rdlane(e, 4 * q + i); a[q >> 2] = __builtin_elementwise_fma(f32x2{ws, ws}, __builtin_bit_cast(f32x2, gr[4 * q + i]), a[q >> 2]); }
#define GF(q) R4(GF1, q)
    R8(GF)
    const float i0 = rdlane(inv, 0), i1 = rdlane(inv, 16);
    *(LAS unsigned*)(L + L_A + (u * 64 + lane) * 4) = pk_bf(a[0][0] * i0, a[0][1] * i0);
    *(LAS unsigned*)(L + L_A + ASTR + (u * 64 + lane) * 4) = pk_bf(a[1][0] * i1, a[1][1] * i1);
    LBARRIER();
    if (u == 0) STAMP(13);
    const float v = gemv16(L + L_A, lane, bf);
    if (lane < 32) __builtin_amdgcn_raw_buffer_store_b32(tagw(v, tb), rsx, WS_HP + (kvh * 32 + sub * 4 + u) * 128 + lane * 4, 0, AUX_SC1);
    if (u == 0) STAMP(7);
    continue;
  }
  if (!live) { }
  else if (u == 0) {
    lds_wait_ge(ctl + C_PUB, 1u);
#pragma unroll 1
    for (unsigned sp = 0; sp < SPIN_CAP; ++sp) {
      MEMBAR();
      const u32x2 t = __builtin_amdgcn_raw_buffer_load_b64(rsx, ho, 0, AUX_SC1);
      if (__all(t[1] == tag)) break;
      __builtin_amdgcn_s_sleep(6);
    }
    ctl[C_GO] = 1u;
  } else lds_wait_ge(ctl + C_GO, 1u);
  if (job == 2) {
    const int po = WS_HP + u * 1024 + lane * 16;
    u32x4 gp[8];
#define PZ(q) gp[q] = u32x4{0u, 0u, 0u, 0u};
    R8(PZ)
    unsigned pend = 0xffu;
#pragma unroll 1
    for (unsigned sp = 0; sp < SPIN_CAP; ++sp) {
#define PL(q) if (pend & (1u << q)) POLL4(gp[q], po, rsp, q * 4096);
      R8(PL)
      WAIT_VM();
#define PC(q) if (pend & (1u << q)) { asm volatile("" : "+v"(gp[q])); gp[q] ^= tb; if (__all(((gp[q][0] | gp[q][1] | gp[q][2] | gp[q][3]) & 0xffu) == 0u)) pend &= ~(1u << q); }
      R8(PC)
      if (pend == 0 || !live) break;
    }
    if (u == 0) STAMP(8);
    {
      f32x4 at = __builtin_bit_cast(f32x4, gp[0]);
#define PA(q) if (q > 0) at += __builtin_bit_cast(f32x4, gp[q]);
      R8(PA)
      const unsigned a01 = pk_bf(at[0], at[1]), a23 = pk_bf(at[2], at[3]);
      const unsigned h01 = pk_bf(bf_lo(xr[0]) + bf_lo(a01), bf_hi(xr[0]) + bf_hi(a01)), h23 = pk_bf(bf_lo(xr[1]) + bf_lo(a23), bf_hi(xr[1]) + bf_hi(a23));
      *(LAS u32x2*)(L + L_A + ((lane >> 2) & 1) * ASTR + (u * 128 + (lane >> 3) * 16 + (lane & 3) * 4) * 2) = u32x2{h01, h23};
      const float pv = bf_lo((lane & 4) ? __builtin_amdgcn_readlane(h01, 4) : __builtin_amdgcn_readlane(h01, 0));
      const float d0 = bf_lo(h01) - pv, d1 = bf_hi(h01) - pv, d2 = bf_lo(h23) - pv, d3 = bf_hi(h23) - pv;
      float t = add32((d0 + d1) + (d2 + d3), fmaf(d0, d0, d1 * d1) + fmaf(d2, d2, d3 * d3));
      t = add16(t, t);
      t += dpp<XOR1>(t); t += dpp<XOR2>(t); t += dpp<ROR8>(t);
      if ((lane & 27) == 0) *(LAS float*)(L + L_RED + ((lane >> 2) & 1) * 64 + u * 16 + (lane >> 5) * 4) = t;
      if ((lane & 59) == 1) *(LAS float*)(L + L_RED + ((lane >> 2) & 1) * 64 + u * 16 + 8) = pv;
    }
    LBARRIER();
    if (u == 0) STAMP(14);
    f32x4 st[4];
#pragma unroll
    for (int k = 0; k < 4; ++k) st[k] = *(LAS f32x4*)(L + L_RED + ((lane >> 4) & 1) * 64 + k * 16);
    const float dv = gemv16(L + L_A, lane, bf);
    float mk[4], ss = 0.f;
#pragma unroll
    for (int k = 0; k < 4; ++k) { mk[k] = fmaf(st[k][0], 1.0f / 128, st[k][2]); ss += fmaf(st[k][0] * (-1.0f / 128), st[k][0], st[k][1]); }
    const float mean = 0.25f * ((mk[0] + mk[1]) + (mk[2] + mk[3]));
#pragma unroll
    for (int k = 0; k < 4; ++k) { const float dm = mk[k] - mean; ss = fmaf(128.0f * dm, dm, ss); }
    const float mb = rbf(mean), rd = __builtin_amdgcn_rcpf(rbf(__builtin_amdgcn_sqrtf(rbf(rbf(fmaxf(ss, 0.f) * (1.0f / DM)) + 1e-5f))));
    const float gv = rbf(fmaf(-mb, cs, dv) * rd);
    const float uv = dpp<ROR8>(gv);
    const float sg = rbf(gv * __builtin_amdgcn_rcpf(1.0f + __builtin_amdgcn_exp2f(-gv * LOG2E)));
    const float av = sg * uv;
    const float nb = dpp<XOR1>(av);
    if (lane < 32 && (lane & 9) == 0) *(LAS unsigned*)(L + L_ACT + (lane >> 4) * 64 + u * 16 + (lane & 7) * 2) = pk_bf(av, nb);
    LBARRIER();
    if (u == 0) STAMP(16);
    const u32x4 aa = *(LAS u32x4*)(L + L_ACT + ((lane >> 2) & 1) * 64 + (lane >> 4) * 16);
    u32x4 o;
#pragma unroll
    for (int i = 0; i < 4; ++i) {
      const float e0 = mfma32(aa, bd[i], f32x4{0.f, 0.f, 0.f, 0.f})[0], e1 = mfma32(aa, bd[4 + i], f32x4{0.f, 0.f, 0.f, 0.f})[0];
      o[i] = tagw((lane & 32) ? e1 : e0, tb);
    }
    __builtin_amdgcn_raw_buffer_store_b128(o, rsx, WS_DP + fj * 4096 + u * 1024 + lane * 16, 0, AUX_SC1);
    if (u == 0) STAMP(9);
  } else {
    const int hpo = WS_HP + (ocol >> 4) * 128 + ((rho >> 2) & 1) * 64 + (ocol & 15) * 4;
    unsigned gp[8];
#pragma unroll 1
    for (unsigned sp = 0; sp < SPIN_CAP; ++sp) {
      MEMBAR();
#pragma unroll
      for (int s = 0; s < 8; ++s) gp[s] = __builtin_amdgcn_raw_buffer_load_b32(rsx, hpo + s * 4096, 0, AUX_SC1) ^ tb;
      unsigned bad = 0;
#pragma unroll
      for (int s = 0; s < 8; ++s) bad |= gp[s];
      if (__all((bad & 0xffu) == 0u) || !live) break;
      __builtin_amdgcn_s_sleep(4);
    }
    float s0 = __uint_as_float(gp[0]);
#pragma unroll
    for (int s = 1; s < 8; ++s) s0 += __uint_as_float(gp[s]);
    const float hres = rbf(bf_lo(xr[0]) + rbf(s0));
    u32x4 ga[4];
    const int ao = WS_DP + (lane >> 2) * 4096 + rho * 64 + (lane & 3) * 16;
    const int ao3 = lane < 32 ? ao + 3 * 65536 : WS_SPAN;
    const unsigned tb3 = lane < 32 ? tb : 0u;
#pragma unroll 1
    for (unsigned sp = 0; sp < SPIN_CAP; ++sp) {
      MEMBAR();
#pragma unroll
      for (int i = 0; i < 3; ++i) ga[i] = __builtin_amdgcn_raw_buffer_load_b128(rsx, ao, i * 65536, AUX_SC1) ^ tb;
      ga[3] = __builtin_amdgcn_raw_buffer_load_b128(rsx, ao3, 0, AUX_SC1) ^ tb3;
      const u32x4 bad = ga[0] | ga[1] | ga[2] | ga[3];
      if (__all(((bad[0] | bad[1] | bad[2] | bad[3]) & 0xffu) == 0u) || !live) break;
    }
    STAMP(10);
    const f32x4 y4 = (__builtin_bit_cast(f32x4, ga[0]) + __builtin_bit_cast(f32x4, ga[1])) + (__builtin_bit_cast(f32x4, ga[2]) + __builtin_bit_cast(f32x4, ga[3]));
    float y = add16(add32(y4[0], y4[1]), add32(y4[2], y4[3]));
    y += dpp<ROR8>(y); y += dpp<ROR4>(y);
    const unsigned ob = pk_bf(hres + rbf(y), 0.f);
    const __amdgpu_buffer_rsrc_t rso = __builtin_amdgcn_make_buffer_rsrc((void*)p.out, 0, live ? 2048 : 0, RSRC3);
    if ((lane & 12) == 0) __builtin_amdgcn_raw_buffer_store_b16((u16)ob, rso, (((rho >> 2) & 1) * 512 + ocol) * 2, 0, AUX_SC1);
    if (rho == 0 && lane == 0) __builtin_amdgcn_raw_buffer_store_b32(tag, rsx, WS_EPOCH, 0, AUX_SC1);
    STAMP(11);
  }
  }
  EXIT();
  }

  if (w == 0) STAMP(0);
  const int r = lane >> 4, c = lane & 15;
  const unsigned sh = p.share[g];
  const int ns = sh & 0xffff;
  const size_t base = (size_t)grp * SK * 256 + (size_t)(sh >> 16) * 4096;
  u32x4 rs = make_srd((const unsigned char*)p.kc + base, ns * 4096);
  const int klane = (c >> 2) * 256 + r * 64 + (c & 3) * 16;
  int off = w * 1024 + klane;
  const int n = (ns + 3) >> 2;
  u32x4 ring[2][4], xk[XW][4];
  if (hasq) {
    const u32x4 rsq = make_srd(p.wq, 512 * 8192);
    const int qo = (lane >> 2) * 8192 + (((lane & 3) ^ ((lane >> 4) & 2)) << 4);
    int so = w * 128 * 8192 + (kvh * 512 + qt * 32) * 2;
    unsigned dst = (unsigned)(size_t)(L + L_SLAB) + w * 8192;
#pragma unroll 1
    for (int i = 0; i < 8; ++i) {
      unsigned keep;
      asm volatile("s_mov_b32 %0, m0\n\ts_mov_b32 m0, %1\n\ts_nop 0\n\tbuffer_load_dwordx4 %2, %3, %4 offen nt lds\n\ts_mov_b32 m0, %0"
                   : "=&s"(keep) : "s"(dst), "v"(qo), "s"(rsq), "s"(so) : "memory");
      so += 16 * 8192; dst += 1024;
    }
  }
  __builtin_amdgcn_sched_barrier(0);
#define FIRST(j, i) LD1I(xk[j][i], off, rs, i);
#define AHEAD1(j, i) asm volatile("s_waitcnt vmcnt(" STR(PACE) ")"); LD1I(xk[j][i], off, rs, i);
#define AHEAD(j) if (j > 0) { R4(AHEAD1, j) off += 16384; }
#define RING1(j, i) asm volatile("s_waitcnt vmcnt(" STR(PACE) ")"); LD1I(ring[j][i], off, rs, i);
  R4(FIRST, 0)
  off += 16384;
  __builtin_amdgcn_s_barrier();
  if (hasq) { asm volatile("s_waitcnt vmcnt(4)" ::: "memory"); (void)lds_ticket(ctl_addr + C_HS * 4); }
  R8(AHEAD)
  R4(RING1, 0)
  off += 16384;
  R4(RING1, 1)
  off += 16384;
  __builtin_amdgcn_sched_barrier(0);
  ctl[C_WIN] = 1u;
  lds_wait_ge(ctl + C_QRDY, 1u);
  const u32x4 qf = *(LAS u32x4*)(L + L_Q + klane);
  asm volatile("s_waitcnt vmcnt(8)");
#define PINX(x) PIN4(xk[x][0], xk[x][1], xk[x][2], xk[x][3]);
  R8(PINX)
  if (w == 0) STAMP(1);
  const unsigned mask0 = (c & 3) == 0 ? 0x0000ffffu : ((c & 3) == 1 ? 0xffff0000u : 0u);
  const unsigned mask1 = (c & 3) == 2 ? 0x0000ffffu : ((c & 3) == 3 ? 0xffff0000u : 0u);
  const float SCALE = 0.08838834764831845f;
  LAS unsigned char* sc = L + L_SC + w * (40 * 128) + (lane >> 2) * 8;
  float mL = -1.0e38f, l = 0.f;
  f32x4 o1 = {0.f, 0.f, 0.f, 0.f}, o2 = {0.f, 0.f, 0.f, 0.f};
  auto scores = [&](const f32x4 (&d)[4], int t) {
    float S[4];
#pragma unroll
    for (int i = 0; i < 4; ++i) {
      float x = (c & 2) ? ((c & 1) ? d[i][3] : d[i][2]) : ((c & 1) ? d[i][1] : d[i][0]);
      x += dpp<XOR1>(x); x += dpp<XOR2>(x);
      S[i] = x;
    }
    const unsigned a01 = pk_bf(S[0], S[1]), a23 = pk_bf(S[2], S[3]);
    unsigned b01 = pk_bf(bf_lo(a01) * SCALE, bf_hi(a01) * SCALE), b23 = pk_bf(bf_lo(a23) * SCALE, bf_hi(a23) * SCALE);
    if (4 * t + 4 > ns) {
      const int rem = ns - 4 * t;
      b01 = rem > 1 ? b01 : (rem > 0 ? (b01 & 0xffffu) | 0xff800000u : 0xff80ff80u);
      b23 = rem > 2 ? (b23 & 0xffffu) | 0xff800000u : 0xff80ff80u;
    }
    mL = fmaxf(fmaxf(mL, fmaxf(bf_lo(b01), bf_hi(b01))), fmaxf(bf_lo(b23), bf_hi(b23)));
    *(LAS u32x2*)(sc + t * 128) = u32x2{b01, b23};
  };
  const int nk = n + ((n - XW) & 1);
#pragma unroll 1
  for (int step = XW; step < nk; step += 2) {
    if (step + 2 == nk) {
      rs = make_srd((const unsigned char*)p.vc + base, ns * 4096);
      off = w * 1024 + lane * 16 - (nk - n) * 16384;
    }
    f32x4 d[4];
#define KLD(j, i) WAITP(ring[j][i]); d[i] = mfma32(ring[j][i], qf, f32x4{0.f, 0.f, 0.f, 0.f}); LD1(ring[j][i], off, rs, i);
#define XMM(x, i) d[i] = mfma32(xk[x][i], qf, f32x4{0.f, 0.f, 0.f, 0.f});
#define XTILE(j, y) if (step == XW + 2 * y) { R4(XMM, 2 * y + j) scores(d, 2 * y + j); }
#define KSTEP(j) R4(KLD, j) off += 16384; scores(d, step + j); if (step < 2 * XW) { XTILE(j, 0) XTILE(j, 1) XTILE(j, 2) XTILE(j, 3) }
    KSTEP(0)
    KSTEP(1)
  }
  mL = x16_32_max(mL) * LOG2E;
  if (w == 0) STAMP(17);
  u32x2 bp[4];
  auto probs = [&](int t) {
    const u32x2 sv = *(LAS u32x2*)(sc + t * 128);
    const float p0 = __builtin_amdgcn_exp2f(fmaf(bf_lo(sv[0]), LOG2E, -mL)), p1 = __builtin_amdgcn_exp2f(fmaf(bf_hi(sv[0]), LOG2E, -mL));
    const float p2 = __builtin_amdgcn_exp2f(fmaf(bf_lo(sv[1]), LOG2E, -mL)), p3 = __builtin_amdgcn_exp2f(fmaf(bf_hi(sv[1]), LOG2E, -mL));
    l += (p0 + p1) + (p2 + p3);
    const unsigned b0 = pk_bf(p0, p0), b1 = pk_bf(p1, p1), b2 = pk_bf(p2, p2), b3 = pk_bf(p3, p3);
    bp[0] = u32x2{b0 & mask0, b0 & mask1}; bp[1] = u32x2{b1 & mask0, b1 & mask1}; bp[2] = u32x2{b2 & mask0, b2 & mask1}; bp[3] = u32x2{b3 & mask0, b3 & mask1};
    PIN4(bp[0], bp[1], bp[2], bp[3]);
  };
#define VLD(j, i) WAITP(ring[j][i]); o1 = mfma16(u32x2{ring[j][i][0], ring[j][i][1]}, bp[i], o1); o2 = mfma16(u32x2{ring[j][i][2], ring[j][i][3]}, bp[i], o2); LD1(ring[j][i], off, rs, i);
#pragma unroll 1
  for (int t = n - nk; t < n; t += 2) {
    probs(t < 0 ? n : t);
    R4(VLD, 0)
    off += 16384;
    probs(t + 1);
    R4(VLD, 1)
    off += 16384;
  }
  STAMP(2);
  l = x16_32_sum(l);
  LAS float* mg = (LAS float*)(L + L_MRG) + lane;
#pragma unroll
  for (int i = 0; i < 4; ++i) { mg[w * 640 + i * 64] = o1[i]; mg[w * 640 + (4 + i) * 64] = o2[i]; }
  mg[w * 640 + 512] = mL; mg[w * 640 + 576] = l;
  WAIT_LDS();
  if (lds_ticket(ctl_addr + C_MRG * 4) == 3) {
    const unsigned tag = ctl[C_TAG], tb = tag & 0xffu;
    const __amdgpu_buffer_rsrc_t rsw = __builtin_amdgcn_make_buffer_rsrc((void*)p.ws, 0, WS_SPAN, RSRC3);
    float mv[4][10];
#pragma unroll
    for (int u = 0; u < 4; ++u)
#pragma unroll
      for (int i = 0; i < 10; ++i) mv[u][i] = mg[u * 640 + i * 64];
    __builtin_amdgcn_sched_barrier(0x2 | 0x4);
    const float M = fmaxf(fmaxf(mv[0][8], mv[1][8]), fmaxf(mv[2][8], mv[3][8]));
    float Lt = 0.f, acc[8];
#pragma unroll
    for (int i = 0; i < 8; ++i) acc[i] = 0.f;
#pragma unroll
    for (int u = 0; u < 4; ++u) {
      const float f = __builtin_amdgcn_exp2f(mv[u][8] - M);
      Lt = fmaf(f, mv[u][9], Lt);
#pragma unroll
      for (int i = 0; i < 8; ++i) acc[i] = fmaf(f, mv[u][i], acc[i]);
    }
    const float inv = __builtin_amdgcn_rcpf(Lt);
    const int rec = bat * 16 + mem, h = c >> 2;
    const int ro = WS_REC + (kvh * 32 + rec) * 2048 + h * 512 + (r * 4 + (c & 3)) * 16;
#pragma unroll
    for (int i = 0; i < 2; ++i)
      __builtin_amdgcn_raw_buffer_store_b128(u32x4{tagw(acc[2 * i] * inv, tb), tagw(acc[4 + 2 * i] * inv, tb), tagw(acc[2 * i + 1] * inv, tb), tagw(acc[5 + 2 * i] * inv, tb)}, rsw, ro + i * 256, 0, AUX_SC1);
    if (r == 0 && (c & 3) == 0)
      __builtin_amdgcn_raw_buffer_store_b64(u32x2{__float_as_uint(M + __builtin_amdgcn_logf(Lt)), tag}, rsw, WS_HDR + (kvh * 32 + rec) * 128 + h * 8, 0, AUX_SC1);
    MEMBAR();
    ctl[C_PUB] = 1u;
    STAMP(3);
  }
  WAIT4(ring[0], 0); WAIT4(ring[1], 0);
  EXIT();
}

extern "C" void kernel_launch(void* const* d_in, const int* in_sizes, int n_in, void* d_out, int out_size, void* d_ws, size_t ws_size, hipStream_t stream) {
  Params p;
  p.x = (const u16*)d_in[0]; p.kc = (const u16*)d_in[1]; p.vc = (const u16*)d_in[2]; p.wq = (const u16*)d_in[3]; p.wo = (const u16*)d_in[4];
  p.wg = (const u16*)d_in[6]; p.wu = (const u16*)d_in[7]; p.wd = (const u16*)d_in[8];
  p.out = (u16*)d_out; p.ws = (unsigned char*)d_ws;
  for (int grp = 0; grp < 16; ++grp) {
    int cost[16], sum = 0;
    for (int m = 0; m < 16; ++m) {
      const int jb = (m + grp) & 3, ji = m >> 2, bat = grp >> 3;
      int c;
      if (jb == 1) c = C1 + (bat == 0 && ji == 3 ? CS1 : 0);
      else if (jb == 2) c = ((grp & 3) == 3 && (ji & 1)) ? 0 : C2 + (((grp * 2 + (ji & 1) - (grp >> 2)) & 3) == 3 ? CS2 : 0);
      else c = CQ + (jb == 3 && bat == 0 && (ji & 1) ? CSQ : 0);
      c += (m & 1) ? PAR : -(PAR);
      cost[m] = c; sum += c;
    }
    const int tot = 2048 + sum, lvl = tot / 16;
    int rem = tot - lvl * 16, first = 0;
    for (int m = 0; m < 16; ++m) {
      int n = lvl - cost[m];
      if (rem > 0 && (((m * 7) & 15) < tot - lvl * 16)) { ++n; --rem; }
      p.share[grp * 16 + m] = (unsigned)first << 16 | (unsigned)n;
      first += n;
    }
  }
  (void)hipFuncSetAttribute((const void*)fused_decode, hipFuncAttributeMaxDynamicSharedMemorySize, L_BYTES);
  hipLaunchKernelGGL(fused_decode, dim3(NWG), dim3(NTHR), L_BYTES, stream, p);
}
```

```cpp
#define PROBE 0
#include <hip/hip_runtime.h>
#include <stdint.h>

#define STAMPS 0
#define PACE 5
#define PACE0 PACE
#define QLATE 0
#define QDELAY 400
#define SPEC 3
#define XW 10
#define OWNSPEC 1
#define DRY 0
#define C1 8
#define C2 15
#define CQ 4
#define CS1 3
#define CS2 4
#define CSQ 3
#define TOUCH 0
#define CODEW 32
#define CODEL 0
#define PAR 1

typedef unsigned short u16;
typedef unsigned long long u64;
typedef unsigned u32x4 __attribute__((ext_vector_type(4)));
typedef unsigned u32x2 __attribute__((ext_vector_type(2)));
typedef float f32x4 __attribute__((ext_vector_type(4)));
typedef __bf16 bf16x8_t __attribute__((ext_vector_type(8)));
typedef __bf16 bf16x2_t __attribute__((ext_vector_type(2)));
typedef float f32x2 __attribute__((ext_vector_type(2)));
typedef short s16x4 __attribute__((ext_vector_type(4)));
#define GAS __attribute__((address_space(1)))
#define LAS __attribute__((address_space(3)))

constexpr int DM = 512, SK = 32768, FF = 1792;
constexpr int NWG = 256, NTHR = 512;
constexpr int AUX_NT = 2, AUX_SC1 = 16;
constexpr int RSRC3 = 0x00020000;
constexpr unsigned SPIN_CAP = 1u << 18;

constexpr int WS_EPOCH = 0;
constexpr int WS_Q = 8192;
constexpr int WS_HDR = WS_Q + 32768;
constexpr int WS_REC = WS_HDR + 32768;
constexpr int WS_HP = WS_REC + 524288;
constexpr int WS_DP = WS_HP + 32768;
constexpr int WS_SPAN = 2 << 20;
constexpr int WS_STAMP = 4 << 20;
static_assert(WS_DP + 56 * 4096 <= WS_SPAN, "ws map");

constexpr int ASTR = 1056;
constexpr int L_SLAB = 0;
constexpr int L_Q = 98304;
constexpr int L_A = L_Q + 1024;
constexpr int L_ACT = L_A + 2 * ASTR;
constexpr int L_MRG = L_ACT + 128;
constexpr int L_RED = L_MRG + 4 * 2560;
constexpr int L_CNT = L_RED + 128;
constexpr int L_SC = L_CNT + 64;
constexpr int L_SINK = L_SC + 4 * 40 * 128;
constexpr int L_BYTES = L_SINK + 256;
constexpr int CODE_BYTES = 23552;
constexpr int C_QRDY = 0, C_TAG = 1, C_MRG = 2, C_HS = 3, C_GO = 4, C_WIN = 5, C_PUB = 6;

struct Params {
  const u16 *x, *kc, *vc, *wq, *wo, *wg, *wu, *wd;
  u16* out;
  unsigned char* ws;
  unsigned share[NWG];
};

__device__ __forceinline__ float bf_lo(unsigned u) { return __builtin_bit_cast(float, u << 16); }
__device__ __forceinline__ float bf_hi(unsigned u) { return __builtin_bit_cast(float, u & 0xffff0000u); }
__device__ __forceinline__ unsigned pk_bf(float lo, float hi) {
  bf16x2_t r = __builtin_convertvector(f32x2{lo, hi}, bf16x2_t);
  return __builtin_bit_cast(unsigned, r);
}
__device__ __forceinline__ float rbf(float f) { return bf_lo(pk_bf(f, f)); }
template <int CTRL> __device__ __forceinline__ float dpp(float x) {
  return __builtin_bit_cast(float, __builtin_amdgcn_mov_dpp(__builtin_bit_cast(int, x), CTRL, 0xf, 0xf, true));
}
constexpr int XOR1 = 0xB1, XOR2 = 0x4E, HMIR = 0x141, MIR = 0x140, ROR8 = 0x128, ROR4 = 0x124;
__device__ __forceinline__ float x16_32_max(float x) {
  auto s = __builtin_amdgcn_permlane16_swap(__float_as_uint(x), __float_as_uint(x), false, false);
  x = fmaxf(__uint_as_float(s[0]), __uint_as_float(s[1]));
  auto t = __builtin_amdgcn_permlane32_swap(__float_as_uint(x), __float_as_uint(x), false, false);
  return fmaxf(__uint_as_float(t[0]), __uint_as_float(t[1]));
}
__device__ __forceinline__ float add16(float a, float b) {
  auto s = __builtin_amdgcn_permlane16_swap(__float_as_uint(a), __float_as_uint(b), false, false);
  return __uint_as_float(s[0]) + __uint_as_float(s[1]);
}
__device__ __forceinline__ float add32(float a, float b) {
  auto s = __builtin_amdgcn_permlane32_swap(__float_as_uint(a), __float_as_uint(b), false, false);
  return __uint_as_float(s[0]) + __uint_as_float(s[1]);
}
__device__ __forceinline__ float x16_32_sum(float x) { x = add16(x, x); return add32(x, x); }
__device__ __forceinline__ float row_sum(float v) { v += dpp<XOR1>(v); v += dpp<XOR2>(v); v += dpp<HMIR>(v); v += dpp<MIR>(v); return v; }
__device__ __forceinline__ float row_max(float v) { v = fmaxf(v, dpp<XOR1>(v)); v = fmaxf(v, dpp<XOR2>(v)); v = fmaxf(v, dpp<HMIR>(v)); v = fmaxf(v, dpp<MIR>(v)); return v; }
__device__ __forceinline__ float wave_sum(float v) { return x16_32_sum(row_sum(v)); }
__device__ __forceinline__ float rdlane(float v, int l) { return __builtin_bit_cast(float, __builtin_amdgcn_readlane(__builtin_bit_cast(int, v), l)); }
__device__ __forceinline__ f32x4 mfma32(u32x4 a, u32x4 b, f32x4 c) {
  return __builtin_amdgcn_mfma_f32_16x16x32_bf16(__builtin_bit_cast(bf16x8_t, a), __builtin_bit_cast(bf16x8_t, b), c, 0, 0, 0);
}
__device__ __forceinline__ f32x4 mfma16(u32x2 a, u32x2 b, f32x4 c) {
  return __builtin_amdgcn_mfma_f32_16x16x16bf16_1k(__builtin_bit_cast(s16x4, a), __builtin_bit_cast(s16x4, b), c, 0, 0, 0);
}
#define MEMBAR() asm volatile("" ::: "memory")
#define WAIT_LDS() asm volatile("s_waitcnt lgkmcnt(0)" ::: "memory")
#define WAIT_VM() asm volatile("s_waitcnt vmcnt(0)" ::: "memory")
#define LBARRIER() do { WAIT_LDS(); if (live) __builtin_amdgcn_s_barrier(); MEMBAR(); } while (0)
#define BARRIER() do { WAIT_LDS(); __builtin_amdgcn_s_barrier(); MEMBAR(); } while (0)

#define R4(M, a) M(a, 0) M(a, 1) M(a, 2) M(a, 3)
#define R8(M) M(0) M(1) M(2) M(3) M(4) M(5) M(6) M(7)
#define RX(M) R8(M) M(8) M(9)
#define STR2(x) #x
#define STR(x) STR2(x)
#define LD1X(C, d, off, rs) asm volatile("buffer_load_dwordx4 %0, %1, %2, 0 offen nt" : C(d) : "v"(off), "s"(rs))
#define LD1(d, off, rs, i) LD1X("+v", d, (off) + (i) * 4096, rs)
#define LD1I(d, off, rs, i) LD1X("=&v", d, (off) + (i) * 4096, rs)
#define WAITP(d) asm volatile("s_waitcnt vmcnt(" STR(PACE) ")" : "+v"(d))
#define WAIT4(d, n) asm volatile("s_waitcnt vmcnt(" #n ")" : "+v"(d[0]), "+v"(d[1]), "+v"(d[2]), "+v"(d[3]))
static_assert(PACE <= 7 && (XW == 8 || XW == 10), "ring of eight loads; eight or ten tiles ahead");
__device__ __forceinline__ u32x4 make_srd(const void* base, unsigned bytes) {
  const u64 a = (u64)base;
  return u32x4{(unsigned)a, (unsigned)(a >> 32) & 0xffffu, bytes, (unsigned)RSRC3};
}
#define POLL2(d, off, rs, so) asm volatile("buffer_load_dwordx2 %0, %1, %2, %3 offen sc1" : "+v"(d) : "v"(off), "s"(rs), "s"(so) : "memory")
#define POLL4(d, off, rs, so) asm volatile("buffer_load_dwordx4 %0, %1, %2, %3 offen sc1" : "+v"(d) : "v"(off), "s"(rs), "s"(so) : "memory")
#define PIN4(a, b, c, d) asm volatile("" : "+v"(a), "+v"(b), "+v"(c), "+v"(d))

#define STAMP(i) do { } while (0)
#define DUMP() do { } while (0)

#define DMA16(src, dst) __builtin_amdgcn_global_load_lds((const GAS unsigned*)(src), (LAS unsigned*)(dst), 16, 0, AUX_NT)
__device__ __forceinline__ u32x4 tr_frag(LAS unsigned char* a, int plus4) {
  const u32x2 lo = __builtin_bit_cast(u32x2, __builtin_amdgcn_ds_read_tr16_b64_v4i16((LAS s16x4*)a));
  const u32x2 hi = __builtin_bit_cast(u32x2, __builtin_amdgcn_ds_read_tr16_b64_v4i16((LAS s16x4*)(a + plus4)));
  return u32x4{lo[0], lo[1], hi[0], hi[1]};
}
__device__ __forceinline__ float gemv16(LAS unsigned char* avec, int lane, const u32x4 (&bf)[16]) {
  LAS unsigned char* a = avec + (lane & 1) * ASTR + ((lane >> 1) & 7) * 64 + (lane >> 4) * 16;
  const u32x4 a0 = *(LAS u32x4*)a, a1 = *(LAS u32x4*)(a + 512);
  f32x4 d[8];
#pragma unroll
  for (int s = 0; s < 8; ++s) d[s] = mfma32(a0, bf[s], f32x4{0.f, 0.f, 0.f, 0.f});
#pragma unroll
  for (int s = 0; s < 8; ++s) d[s] = mfma32(a1, bf[8 + s], d[s]);
  float v[2];
#pragma unroll
  for (int b = 0; b < 2; ++b) {
    const float t0 = d[0][b] + d[1][2 + b], t1 = d[2][b] + d[3][2 + b], t2 = d[4][b] + d[5][2 + b], t3 = d[6][b] + d[7][2 + b];
    v[b] = (lane & 32) ? ((lane & 16) ? t3 : t2) : ((lane & 16) ? t1 : t0);
  }
  const float x = add16(v[0], v[1]);
  return add32(x, x);
}
__device__ __forceinline__ unsigned tagw(float v, unsigned tb) { return (__float_as_uint(v) & 0xffffff00u) | tb; }
__device__ __forceinline__ void lds_wait_ge(volatile LAS unsigned* w, unsigned v) {
#pragma unroll 1
  for (unsigned sp = 0; sp < (SPIN_CAP << 4); ++sp) { if ((unsigned)__builtin_amdgcn_readfirstlane(*w) >= v) break; __builtin_amdgcn_s_sleep(1); }
  MEMBAR();
}
__device__ __forceinline__ unsigned lds_ticket(unsigned addr) {
  unsigned r; u64 keep;
  asm volatile("s_mov_b64 %1, exec\n\ts_mov_b64 exec, 1\n\tds_add_rtn_u32 %0, %2, %3\n\ts_mov_b64 exec, %1\n\ts_waitcnt lgkmcnt(0)"
               : "=&v"(r), "=&s"(keep) : "v"(addr), "v"(1u) : "memory");
  return __builtin_amdgcn_readfirstlane(r);
}

#define EXIT() do { DUMP(); __builtin_amdgcn_endpgm(); } while (0)

__device__ __forceinline__ void touch_code(u64 pc, unsigned sink, int lane) {
  unsigned keep;
  const int o0 = lane * 128, o1 = o0 + 8192, o2 = min(o0 + 16384, CODE_BYTES - 128);
  asm volatile("s_mov_b32 %0, m0\n\ts_mov_b32 m0, %1\n\ts_nop 0\n\tglobal_load_lds_dword %2, %5\n\tglobal_load_lds_dword %3, %5\n\tglobal_load_lds_dword %4, %5\n\ts_mov_b32 m0, %0"
               : "=&s"(keep) : "s"(sink), "v"(o0), "v"(o1), "v"(o2), "s"(pc) : "memory");
}

__global__ void __launch_bounds__(NTHR, 2) fused_decode(Params p) {
  u64 t_top; asm volatile("s_memrealtime %0\n\ts_waitcnt lgkmcnt(0)" : "=s"(t_top) :: "memory");
  __builtin_amdgcn_sched_barrier(0);
  u64 pc0;
  asm volatile("s_getpc_b64 %0" : "=s"(pc0));
  pc0 &= ~(u64)255;
  extern __shared__ __attribute__((aligned(16))) unsigned char smem[];
  LAS unsigned char* L = (LAS unsigned char*)smem;
  volatile LAS unsigned* ctl = (volatile LAS unsigned*)(L + L_CNT);
  const unsigned ctl_addr = (unsigned)(size_t)(L + L_CNT);
  const int g = blockIdx.x, tid = threadIdx.x, lane = tid & 63, w = __builtin_amdgcn_readfirstlane(tid >> 6);
  const int grp = g >> 4, mem = g & 15;
  const int kvh = grp & 7, bat = grp >> 3;
  const int jb = (mem + grp) & 3, ji = mem >> 2;
  const bool hasq = jb == 0 || jb == 3;
  const int qt = ((bat * 2 + (jb == 3)) * 2 + (ji & 1)) * 2 + (ji >> 1);
  const float LOG2E = 1.4426950408889634f;
  if (w == 5 && g < CODEW) touch_code(pc0, (unsigned)(size_t)(L + L_SINK), lane);
  if (w == 4) {
    unsigned keep;
    asm volatile("s_mov_b32 %0, m0\n\ts_mov_b32 m0, %1\n\ts_nop 0\n\tglobal_load_lds_dwordx4 %2, %3\n\ts_add_u32 m0, m0, 0x400\n\ts_nop 0\n\tglobal_load_lds_dwordx4 %4, %3\n\t"
                 "s_add_u32 m0, m0, 0x400\n\ts_nop 0\n\tglobal_load_lds_dword %5, %6 sc1\n\ts_mov_b32 m0, %0"
                 : "=&s"(keep) : "s"((unsigned)(size_t)(L + L_MRG)), "v"(lane * 16), "s"(p.x), "v"(lane * 16 + 1024), "v"(0), "s"(p.ws) : "memory", "scc");
  }

  if (__builtin_expect(w >= 4, 0)) {
  __builtin_amdgcn_s_setprio(3);
  const u64 t_start = __builtin_amdgcn_s_memrealtime();
  const int u = w - 4;
  const int sub = bat * 4 + ji;
  const int job = jb == 2 && (grp & 3) == 3 && (ji & 1) ? 4 : jb;
  const int fj = (grp * 2 + (ji & 1) - (grp >> 2)) * 2 + (ji >> 1);
  const int rho = sub * 8 + kvh;
  LAS unsigned char* slab = L + L_SLAB;
  if (u == 0) { if (lane < 16) ctl[lane] = 0u; WAIT_LDS(); }
  __builtin_amdgcn_s_barrier();
  if ((hasq && u > 1) || (job == 4 && u != 0)) EXIT();
  const __amdgpu_buffer_rsrc_t rsw = __builtin_amdgcn_make_buffer_rsrc((void*)p.ws, 0, WS_SPAN, RSRC3);
  u32x4 bf[16], bd[8];
  LAS unsigned char* fa = slab + ((lane >> 4) * 8 + ((lane >> 2) & 3)) * 128;
  unsigned tag = 0;
  if (u == 0) {
    WAIT_VM();
    const unsigned ep = *(LAS unsigned*)(L + L_MRG + 2048);
    const u32x4 xv[2] = {*(LAS u32x4*)(L + L_MRG + lane * 16), *(LAS u32x4*)(L + L_MRG + 1024 + lane * 16)};
    if (hasq) {
#pragma unroll
      for (int b = 0; b < 2; ++b) {
        float v[8];
#pragma unroll
        for (int i = 0; i < 4; ++i) { v[2 * i] = bf_lo(xv[b][i]); v[2 * i + 1] = bf_hi(xv[b][i]); }
        float s = 0.f;
#pragma unroll
        for (int i = 0; i < 8; ++i) s += v[i];
        const float mean = wave_sum(s) * (1.0f / DM);
        float q2 = 0.f;
#pragma unroll
        for (int i = 0; i < 8; ++i) q2 = fmaf(v[i] - mean, v[i] - mean, q2);
        const float var = wave_sum(q2) * (1.0f / DM);
        const float mb = rbf(mean), rd = __builtin_amdgcn_rcpf(rbf(__builtin_amdgcn_sqrtf(rbf(rbf(var) + 1e-5f))));
        u32x4 o;
#pragma unroll
        for (int i = 0; i < 4; ++i) o[i] = pk_bf(rbf(v[2 * i] - mb) * rd, rbf(v[2 * i + 1] - mb) * rd);
        *(LAS u32x4*)(L + L_A + b * ASTR + lane * 16) = o;
      }
    }
    tag = __builtin_amdgcn_readfirstlane(ep) + 1u;
    ctl[C_TAG] = tag;
    if (hasq) { WAIT_LDS(); (void)lds_ticket(ctl_addr + C_HS * 4); }
  }
  if (hasq) {
    lds_wait_ge(ctl + C_HS, 5u);
    if (u == 0) STAMP(12);
    tag = __builtin_amdgcn_readfirstlane(ctl[C_TAG]);
    LAS unsigned char* fq = slab + ((lane >> 4) * 8 + ((lane >> 2) & 3)) * 64 + (((lane >> 4) & 1) ^ u) * 32 + (lane & 3) * 8;
#pragma unroll
    for (int s = 0; s < 16; ++s) bf[s] = tr_frag(fq + s * 2048, 256);
    const float v = gemv16(L + L_A, lane, bf);
    const float nb = dpp<XOR1>(v);
    if (lane < 32 && !(lane & 1))
      __builtin_amdgcn_raw_buffer_store_b64(u32x2{pk_bf(v, nb), tag}, rsw, WS_Q + ((lane >> 4) * 2048 + kvh * 256 + qt * 16 + u * 8 + ((lane & 15) >> 1)) * 8, 0, AUX_SC1);
    if (u != 0) EXIT();
    STAMP(4);
  }
  if (u == 0) {
    const int qo = WS_Q + (bat * 2048 + kvh * 256 + lane * 2) * 8;
    u32x4 gq[2];
#pragma unroll 1
    for (unsigned sp = 0; sp < 4096u && __builtin_amdgcn_s_memrealtime() - t_start < QDELAY; ++sp) __builtin_amdgcn_s_sleep(2);
#pragma unroll 1
    for (unsigned sp = 0; sp < SPIN_CAP; ++sp) {
      MEMBAR();
#pragma unroll
      for (int i = 0; i < 2; ++i) gq[i] = __builtin_amdgcn_raw_buffer_load_b128(rsw, qo + i * 1024, 0, AUX_SC1);
      const bool ok = gq[0][1] == tag && gq[0][3] == tag && gq[1][1] == tag && gq[1][3] == tag;
      if (__all(ok)) break;
      __builtin_amdgcn_s_sleep(8);
    }
#pragma unroll
    for (int i = 0; i < 2; ++i) *(LAS u32x2*)(L + L_Q + (i * 128 + lane * 2) * 4) = u32x2{gq[i][0], gq[i][2]};
    WAIT_LDS();
    ctl[C_QRDY] = 1u;
    STAMP(5);
    if (TOUCH) {
      const unsigned sh = p.share[g];
      const size_t b0 = (size_t)grp * SK * 256 + (size_t)(sh >> 16) * 4096, b1 = b0 + (size_t)(sh & 0xffff) * 4096 - 128;
      const GAS unsigned char* a = lane == 0 ? (const GAS unsigned char*)p.vc + b0 : (lane == 1 ? (const GAS unsigned char*)p.vc + b1 : (lane == 2 ? (const GAS unsigned char*)p.kc + b1 : (const GAS unsigned char*)p.out));
      if (lane < (job == 3 ? 4 : 3)) { const unsigned t = __builtin_nontemporal_load((const GAS unsigned*)a); asm volatile("" :: "v"(t)); }
    }
    if (job == 0 || job == 4) EXIT();
  } else lds_wait_ge(ctl + C_WIN, 1u);
  float cs = 0.f;
  u32x2 xr = {0u, 0u};
  const int ocol = (rho >> 3) * 64 + ((((lane >> 4) & 1) << 1) | (lane >> 5)) * 16 + (rho & 3) * 4 + (lane & 3);
  if (job == 1) {
    const unsigned char* src = (const unsigned char*)p.wo + (size_t)(kvh * 512 + u * 128 + 4 * ((lane >> 3) & 3) + (lane >> 5)) * 1024 + sub * 128 + (lane & 7) * 16;
#pragma unroll
    for (int i = 0; i < 16; ++i) { DMA16(src + (32 * ((i >> 1) & 3) + 16 * (i >> 3) + 2 * (i & 1)) * 1024, slab + (u * 16 + i) * 1024); }
  } else if (job == 2) {
    xr = *(const GAS u32x2*)((const GAS unsigned char*)p.x + ((lane >> 2) & 1) * 1024 + (u * 128 + (lane >> 3) * 16 + (lane & 3) * 4) * 2);
    const unsigned char* src = (const unsigned char*)((lane & 4) ? p.wu : p.wg) + (size_t)(u * 128 + (lane >> 3)) * (FF * 2) + fj * 64 + (lane & 3) * 16;
#pragma unroll
    for (int i = 0; i < 16; ++i) { DMA16(src + (size_t)i * 8 * FF * 2, slab + (u * 16 + i) * 1024); }
    const unsigned char* srd = (const unsigned char*)p.wd + (size_t)(fj * 32 + u * 8) * 1024 + lane * 16;
#pragma unroll
    for (int i = 0; i < 8; ++i) DMA16(srd + i * 1024, slab + 65536 + (u * 8 + i) * 1024);
  } else if (job == 3) {
    xr[0] = *(const GAS u16*)((const GAS unsigned char*)p.x + ((rho >> 2) & 1) * 1024 + ocol * 2);
  }
  if (job == 1 || job == 2) {
    WAIT_VM();
    (void)lds_ticket(ctl_addr + C_HS * 4);
    lds_wait_ge(ctl + C_HS, 4u);
    tag = __builtin_amdgcn_readfirstlane(ctl[C_TAG]);
    const int co = job == 1 ? u * 32 + (lane & 3) * 8 : (lane & 2) * 32 + u * 16 + (lane & 1) * 8;
#pragma unroll
    for (int s = 0; s < 16; ++s) bf[s] = tr_frag(fa + co + s * 4096, 512);
    if (job == 2) {
      LAS unsigned char* fd = slab + 65536 + ((lane >> 4) * 8 + ((lane >> 2) & 3)) * 1024 + u * 256 + (lane & 3) * 8;
#pragma unroll
      for (int t = 0; t < 8; ++t) bd[t] = tr_frag(fd + t * 32, 4096);
      f32x4 d = {0.f, 0.f, 0.f, 0.f};
#pragma unroll
      for (int s = 0; s < 16; ++s) d = mfma32(u32x4{0x3f803f80u, 0x3f803f80u, 0x3f803f80u, 0x3f803f80u}, bf[s], d);
      cs = d[0];
    }
  }
  const unsigned tb = tag & 0xffu;
  if (CODEL && u == 1 && g < 64) {
#pragma unroll 1
    for (unsigned sp = 0; sp < 100000u && __builtin_amdgcn_s_memrealtime() - t_start < CODEL; ++sp) __builtin_amdgcn_s_sleep(16);
    touch_code(pc0, (unsigned)(size_t)(L + L_SINK), lane);
  }
  const int ho = WS_HDR + (kvh * 32 + (lane & 31)) * 128 + u * 8;
#pragma unroll 1
  for (int pass = DRY ? 0 : 1; pass < 2; ++pass) {
  int live = pass;
  if (DRY) asm volatile("" : "+s"(live));
  const __amdgpu_buffer_rsrc_t rsx = __builtin_amdgcn_make_buffer_rsrc((void*)p.ws, 0, live ? WS_SPAN : 0, RSRC3);
  const u32x4 rsp = make_srd(p.ws, live ? WS_SPAN : 0);
  if (job == 1) {
    const int ro = WS_REC + kvh * 65536 + u * 512 + lane * 8;
    u32x2 hd = {0u, 0u}, gr[32];
#define GZ1(q, i) gr[4 * q + i] = u32x2{0u, 0u};
#define GZ(q) R4(GZ1, q)
    R8(GZ)
    if (live) lds_wait_ge(ctl + C_PUB, 1u);
    unsigned pend = 0xffu, want = OWNSPEC ? 0xffu : 0u;
#pragma unroll 1
    for (unsigned sp = 0; sp < SPIN_CAP; ++sp) {
      POLL2(hd, ho, rsp, 0);
#define GL1(q, i) POLL2(gr[4 * q + i], ro, rsp, (4 * q + i) * 2048);
#define GL(q) if (want & (1u << q)) { R4(GL1, q) }
      R8(GL)
      asm volatile("s_waitcnt vmcnt(0)" : "+v"(hd) :: "memory");
#define GC1(q, i) gr[4 * q + i] ^= tb; bad |= gr[4 * q + i][0] | gr[4 * q + i][1];
#define GC(q) if (want & (1u << q)) { PIN4(gr[4 * q], gr[4 * q + 1], gr[4 * q + 2], gr[4 * q + 3]); unsigned bad = 0; R4(GC1, q) if (__all((bad & 0xffu) == 0u)) pend &= ~(1u << q); }
      R8(GC)
      unsigned hm = (unsigned)__ballot(hd[1] == tag);
      if ((pend == 0 && hm == 0xffffffffu) || !live) break;
      const int miss = 32 - __builtin_popcount(hm);
      if (miss <= SPEC || OWNSPEC) want = pend;
      else {
        hm &= hm >> 1; hm &= hm >> 2;
        unsigned rdy = 0;
#pragma unroll
        for (int q = 0; q < 8; ++q) rdy |= ((hm >> (4 * q)) & 1u) << q;
        want = pend & rdy;
      }
    }
    if (u == 0) STAMP(6);
    const float lse = __uint_as_float(hd[0]);
    const float e = __builtin_amdgcn_exp2f(lse - row_max(lse));
    const float inv = __builtin_amdgcn_rcpf(row_sum(e));
    f32x2 a[2] = {f32x2{0.f, 0.f}, f32x2{0.f, 0.f}};
#define GF1(q, i) { const float ws = rdlane(e, 4 * q + i); a[q >> 2] = __builtin_elementwise_fma(f32x2{ws, ws}, __builtin_bit_cast(f32x2, gr[4 * q + i]), a[q >> 2]); }
#define GF(q) R4(GF1, q)
    R8(GF)
    const float i0 = rdlane(inv, 0), i1 = rdlane(inv, 16);
    *(LAS unsigned*)(L + L_A + (u * 64 + lane) * 4) = pk_bf(a[0][0] * i0, a[0][1] * i0);
    *(LAS unsigned*)(L + L_A + ASTR + (u * 64 + lane) * 4) = pk_bf(a[1][0] * i1, a[1][1] * i1);
    LBARRIER();
    if (u == 0) STAMP(13);
    const float v = gemv16(L + L_A, lane, bf);
    if (lane < 32) __builtin_amdgcn_raw_buffer_store_b32(tagw(v, tb), rsx, WS_HP + (kvh * 32 + sub * 4 + u) * 128 + lane * 4, 0, AUX_SC1);
    if (u == 0 && g == 1) { const unsigned dl = __builtin_amdgcn_readfirstlane(ctl[10]); while (__builtin_amdgcn_s_memrealtime() - t_top < 8000 + (PROBE >= 3 ? 2 : 10) * dl) __builtin_amdgcn_s_sleep(1); }
    continue;
  }
  if (!live) { }
  else if (u == 0) {
    lds_wait_ge(ctl + C_PUB, 1u);
#pragma unroll 1
    for (unsigned sp = 0; sp < SPIN_CAP; ++sp) {
      MEMBAR();
      const u32x2 t = __builtin_amdgcn_raw_buffer_load_b64(rsx, ho, 0, AUX_SC1);
      if (__all(t[1] == tag)) break;
      __builtin_amdgcn_s_sleep(6);
    }
    ctl[C_GO] = 1u;
  } else lds_wait_ge(ctl + C_GO, 1u);
  if (job == 2) {
    const int po = WS_HP + u * 1024 + lane * 16;
    u32x4 gp[8];
#define PZ(q) gp[q] = u32x4{0u, 0u, 0u, 0u};
    R8(PZ)
    unsigned pend = 0xffu;
#pragma unroll 1
    for (unsigned sp = 0; sp < SPIN_CAP; ++sp) {
#define PL(q) if (pend & (1u << q)) POLL4(gp[q], po, rsp, q * 4096);
      R8(PL)
      WAIT_VM();
#define PC(q) if (pend & (1u << q)) { asm volatile("" : "+v"(gp[q])); gp[q] ^= tb; if (__all(((gp[q][0] | gp[q][1] | gp[q][2] | gp[q][3]) & 0xffu) == 0u)) pend &= ~(1u << q); }
      R8(PC)
      if (pend == 0 || !live) break;
    }
    if (u == 0) STAMP(8);
    {
      f32x4 at = __builtin_bit_cast(f32x4, gp[0]);
#define PA(q) if (q > 0) at += __builtin_bit_cast(f32x4, gp[q]);
      R8(PA)
      const unsigned a01 = pk_bf(at[0], at[1]), a23 = pk_bf(at[2], at[3]);
      const unsigned h01 = pk_bf(bf_lo(xr[0]) + bf_lo(a01), bf_hi(xr[0]) + bf_hi(a01)), h23 = pk_bf(bf_lo(xr[1]) + bf_lo(a23), bf_hi(xr[1]) + bf_hi(a23));
      *(LAS u32x2*)(L + L_A + ((lane >> 2) & 1) * ASTR + (u * 128 + (lane >> 3) * 16 + (lane & 3) * 4) * 2) = u32x2{h01, h23};
      const float pv = bf_lo((lane & 4) ? __builtin_amdgcn_readlane(h01, 4) : __builtin_amdgcn_readlane(h01, 0));
      const float d0 = bf_lo(h01) - pv, d1 = bf_hi(h01) - pv, d2 = bf_lo(h23) - pv, d3 = bf_hi(h23) - pv;
      float t = add32((d0 + d1) + (d2 + d3), fmaf(d0, d0, d1 * d1) + fmaf(d2, d2, d3 * d3));
      t = add16(t, t);
      t += dpp<XOR1>(t); t += dpp<XOR2>(t); t += dpp<ROR8>(t);
      if ((lane & 27) == 0) *(LAS float*)(L + L_RED + ((lane >> 2) & 1) * 64 + u * 16 + (lane >> 5) * 4) = t;
      if ((lane & 59) == 1) *(LAS float*)(L + L_RED + ((lane >> 2) & 1) * 64 + u * 16 + 8) = pv;
    }
    LBARRIER();
    if (u == 0) STAMP(14);
    f32x4 st[4];
#pragma unroll
    for (int k = 0; k < 4; ++k) st[k] = *(LAS f32x4*)(L + L_RED + ((lane >> 4) & 1) * 64 + k * 16);
    const float dv = gemv16(L + L_A, lane, bf);
    float mk[4], ss = 0.f;
#pragma unroll
    for (int k = 0; k < 4; ++k) { mk[k] = fmaf(st[k][0], 1.0f / 128, st[k][2]); ss += fmaf(st[k][0] * (-1.0f / 128), st[k][0], st[k][1]); }
    const float mean = 0.25f * ((mk[0] + mk[1]) + (mk[2] + mk[3]));
#pragma unroll
    for (int k = 0; k < 4; ++k) { const float dm = mk[k] - mean; ss = fmaf(128.0f * dm, dm, ss); }
    const float mb = rbf(mean), rd = __builtin_amdgcn_rcpf(rbf(__builtin_amdgcn_sqrtf(rbf(rbf(fmaxf(ss, 0.f) * (1.0f / DM)) + 1e-5f))));
    const float gv = rbf(fmaf(-mb, cs, dv) * rd);
    const float uv = dpp<ROR8>(gv);
    const float sg = rbf(gv * __builtin_amdgcn_rcpf(1.0f + __builtin_amdgcn_exp2f(-gv * LOG2E)));
    const float av = sg * uv;
    const float nb = dpp<XOR1>(av);
    if (lane < 32 && (lane & 9) == 0) *(LAS unsigned*)(L + L_ACT + (lane >> 4) * 64 + u * 16 + (lane & 7) * 2) = pk_bf(av, nb);
    LBARRIER();
    if (u == 0) STAMP(16);
    const u32x4 aa = *(LAS u32x4*)(L + L_ACT + ((lane >> 2) & 1) * 64 + (lane >> 4) * 16);
    u32x4 o;
#pragma unroll
    for (int i = 0; i < 4; ++i) {
      const float e0 = mfma32(aa, bd[i], f32x4{0.f, 0.f, 0.f, 0.f})[0], e1 = mfma32(aa, bd[4 + i], f32x4{0.f, 0.f, 0.f, 0.f})[0];
      o[i] = tagw((lane & 32) ? e1 : e0, tb);
    }
    __builtin_amdgcn_raw_buffer_store_b128(o, rsx, WS_DP + fj * 4096 + u * 1024 + lane * 16, 0, AUX_SC1);
    if (u == 0) STAMP(9);
  } else {
    const int hpo = WS_HP + (ocol >> 4) * 128 + ((rho >> 2) & 1) * 64 + (ocol & 15) * 4;
    unsigned gp[8];
#pragma unroll 1
    for (unsigned sp = 0; sp < SPIN_CAP; ++sp) {
      MEMBAR();
#pragma unroll
      for (int s = 0; s < 8; ++s) gp[s] = __builtin_amdgcn_raw_buffer_load_b32(rsx, hpo + s * 4096, 0, AUX_SC1) ^ tb;
      unsigned bad = 0;
#pragma unroll
      for (int s = 0; s < 8; ++s) bad |= gp[s];
      if (__all((bad & 0xffu) == 0u) || !live) break;
      __builtin_amdgcn_s_sleep(4);
    }
    float s0 = __uint_as_float(gp[0]);
#pragma unroll
    for (int s = 1; s < 8; ++s) s0 += __uint_as_float(gp[s]);
    const float hres = rbf(bf_lo(xr[0]) + rbf(s0));
    u32x4 ga[4];
    const int ao = WS_DP + (lane >> 2) * 4096 + rho * 64 + (lane & 3) * 16;
    const int ao3 = lane < 32 ? ao + 3 * 65536 : WS_SPAN;
    const unsigned tb3 = lane < 32 ? tb : 0u;
#pragma unroll 1
    for (unsigned sp = 0; sp < SPIN_CAP; ++sp) {
      MEMBAR();
#pragma unroll
      for (int i = 0; i < 3; ++i) ga[i] = __builtin_amdgcn_raw_buffer_load_b128(rsx, ao, i * 65536, AUX_SC1) ^ tb;
      ga[3] = __builtin_amdgcn_raw_buffer_load_b128(rsx, ao3, 0, AUX_SC1) ^ tb3;
      const u32x4 bad = ga[0] | ga[1] | ga[2] | ga[3];
      if (__all(((bad[0] | bad[1] | bad[2] | bad[3]) & 0xffu) == 0u) || !live) break;
    }
    STAMP(10);
    const f32x4 y4 = (__builtin_bit_cast(f32x4, ga[0]) + __builtin_bit_cast(f32x4, ga[1])) + (__builtin_bit_cast(f32x4, ga[2]) + __builtin_bit_cast(f32x4, ga[3]));
    float y = add16(add32(y4[0], y4[1]), add32(y4[2], y4[3]));
    y += dpp<ROR8>(y); y += dpp<ROR4>(y);
    const unsigned ob = pk_bf(hres + rbf(y), 0.f);
    const __amdgpu_buffer_rsrc_t rso = __builtin_amdgcn_make_buffer_rsrc((void*)p.out, 0, live ? 2048 : 0, RSRC3);
    if ((lane & 12) == 0) __builtin_amdgcn_raw_buffer_store_b16((u16)ob, rso, (((rho >> 2) & 1) * 512 + ocol) * 2, 0, AUX_SC1);
    if (rho == 0 && lane == 0) __builtin_amdgcn_raw_buffer_store_b32(tag, rsx, WS_EPOCH, 0, AUX_SC1);
    STAMP(11);
  }
  }
  EXIT();
  }

  if (w == 0) STAMP(0);
  const int r = lane >> 4, c = lane & 15;
  const unsigned sh = p.share[g];
  const int ns = sh & 0xffff;
  const size_t base = (size_t)grp * SK * 256 + (size_t)(sh >> 16) * 4096;
  u32x4 rs = make_srd((const unsigned char*)p.kc + base, ns * 4096);
  const int klane = (c >> 2) * 256 + r * 64 + (c & 3) * 16;
  int off = w * 1024 + klane;
  const int n = (ns + 3) >> 2;
  u32x4 ring[2][4], xk[XW][4];
  if (hasq) {
    const u32x4 rsq = make_srd(p.wq, 512 * 8192);
    const int qo = (lane >> 2) * 8192 + (((lane & 3) ^ ((lane >> 4) & 2)) << 4);
    int so = w * 128 * 8192 + (kvh * 512 + qt * 32) * 2;
    unsigned dst = (unsigned)(size_t)(L + L_SLAB) + w * 8192;
#pragma unroll 1
    for (int i = 0; i < 8; ++i) {
      unsigned keep;
      asm volatile("s_mov_b32 %0, m0\n\ts_mov_b32 m0, %1\n\ts_nop 0\n\tbuffer_load_dwordx4 %2, %3, %4 offen nt lds\n\ts_mov_b32 m0, %0"
                   : "=&s"(keep) : "s"(dst), "v"(qo), "s"(rsq), "s"(so) : "memory");
      so += 16 * 8192; dst += 1024;
    }
  }
  __builtin_amdgcn_sched_barrier(0);
#define FIRST(j, i) LD1I(xk[j][i], off, rs, i);
#define AHEAD1(j, i) asm volatile("s_waitcnt vmcnt(" STR(PACE0) ")"); LD1I(xk[j][i], off, rs, i);
#define AHEAD(j) if (j > QLATE) { R4(AHEAD1, j) off += 16384; }
#define RING1(j, i) asm volatile("s_waitcnt vmcnt(" STR(PACE0) ")"); LD1I(ring[j][i], off, rs, i);
  const u64 tA = __builtin_amdgcn_s_memrealtime();
  R4(FIRST, 0)
  off += 16384;
  u64 tB = 0;
  if (PROBE == 2) { asm volatile("s_waitcnt vmcnt(3)"); tB = __builtin_amdgcn_s_memrealtime(); }
  __builtin_amdgcn_s_barrier();
  if (hasq) { asm volatile("s_waitcnt vmcnt(4)" ::: "memory"); (void)lds_ticket(ctl_addr + C_HS * 4); }
  RX(AHEAD)
  R4(RING1, 0)
  off += 16384;
  R4(RING1, 1)
  off += 16384;
  __builtin_amdgcn_sched_barrier(0);
  ctl[C_WIN] = 1u;
  if (w == 0) { if (PROBE == 1) ctl[10] = (unsigned)(tA - t_top); if (PROBE == 2) ctl[10] = (unsigned)(tB - t_top); if (PROBE == 3) ctl[10] = (unsigned)(__builtin_amdgcn_s_memrealtime() - t_top); }
  if (w == 0) STAMP(18);
  lds_wait_ge(ctl + C_QRDY, 1u);
  const u32x4 qf = *(LAS u32x4*)(L + L_Q + klane);
  asm volatile("s_waitcnt vmcnt(8)");
#define PINX(x) PIN4(xk[x][0], xk[x][1], xk[x][2], xk[x][3]);
  RX(PINX)
  if (w == 0) STAMP(1);
  const unsigned mask0 = (c & 3) == 0 ? 0x0000ffffu : ((c & 3) == 1 ? 0xffff0000u : 0u);
  const unsigned mask1 = (c & 3) == 2 ? 0x0000ffffu : ((c & 3) == 3 ? 0xffff0000u : 0u);
  const float SCALE = 0.08838834764831845f;
  LAS unsigned char* sc = L + L_SC + w * (40 * 128) + (lane >> 2) * 8;
  float mL = -1.0e38f, l = 0.f;
  f32x4 o1 = {0.f, 0.f, 0.f, 0.f}, o2 = {0.f, 0.f, 0.f, 0.f};
  auto scores = [&](const f32x4 (&d)[4], int t) {
    float S[4];
#pragma unroll
    for (int i = 0; i < 4; ++i) {
      float x = (c & 2) ? ((c & 1) ? d[i][3] : d[i][2]) : ((c & 1) ? d[i][1] : d[i][0]);
      x += dpp<XOR1>(x); x += dpp<XOR2>(x);
      S[i] = x;
    }
    const unsigned a01 = pk_bf(S[0], S[1]), a23 = pk_bf(S[2], S[3]);
    unsigned b01 = pk_bf(bf_lo(a01) * SCALE, bf_hi(a01) * SCALE), b23 = pk_bf(bf_lo(a23) * SCALE, bf_hi(a23) * SCALE);
    if (4 * t + 4 > ns) {
      const int rem = ns - 4 * t;
      b01 = rem > 1 ? b01 : (rem > 0 ? (b01 & 0xffffu) | 0xff800000u : 0xff80ff80u);
      b23 = rem > 2 ? (b23 & 0xffffu) | 0xff800000u : 0xff80ff80u;
    }
    mL = fmaxf(fmaxf(mL, fmaxf(bf_lo(b01), bf_hi(b01))), fmaxf(bf_lo(b23), bf_hi(b23)));
    *(LAS u32x2*)(sc + t * 128) = u32x2{b01, b23};
  };
  const int nk = n + ((n - XW) & 1);
#pragma unroll 1
  for (int step = XW; step < nk; step += 2) {
    if (step + 2 == nk) {
      rs = make_srd((const unsigned char*)p.vc + base, ns * 4096);
      off = w * 1024 + lane * 16 - (nk - n) * 16384;
    }
    f32x4 d[4];
#define KLD(j, i) WAITP(ring[j][i]); d[i] = mfma32(ring[j][i], qf, f32x4{0.f, 0.f, 0.f, 0.f}); LD1(ring[j][i], off, rs, i);
#define XMM(x, i) d[i] = mfma32(xk[x][i], qf, f32x4{0.f, 0.f, 0.f, 0.f});
#define XTILE(j, y) if (step == XW + 2 * y) { R4(XMM, 2 * y + j) scores(d, 2 * y + j); }
#define XT4(j) XTILE(j, 4)
#define KSTEP(j) R4(KLD, j) off += 16384; scores(d, step + j); if (step < 2 * XW) { XTILE(j, 0) XTILE(j, 1) XTILE(j, 2) XTILE(j, 3) XT4(j) }
    KSTEP(0)
    KSTEP(1)
  }
  mL = x16_32_max(mL) * LOG2E;
  if (w == 0 && PROBE == 4) ctl[10] = (unsigned)(__builtin_amdgcn_s_memrealtime() - t_top);
  u32x2 bp[4];
  auto probs = [&](int t) {
    const u32x2 sv = *(LAS u32x2*)(sc + t * 128);
    const float p0 = __builtin_amdgcn_exp2f(fmaf(bf_lo(sv[0]), LOG2E, -mL)), p1 = __builtin_amdgcn_exp2f(fmaf(bf_hi(sv[0]), LOG2E, -mL));
    const float p2 = __builtin_amdgcn_exp2f(fmaf(bf_lo(sv[1]), LOG2E, -mL)), p3 = __builtin_amdgcn_exp2f(fmaf(bf_hi(sv[1]), LOG2E, -mL));
    l += (p0 + p1) + (p2 + p3);
    const unsigned b0 = pk_bf(p0, p0), b1 = pk_bf(p1, p1), b2 = pk_bf(p2, p2), b3 = pk_bf(p3, p3);
    bp[0] = u32x2{b0 & mask0, b0 & mask1}; bp[1] = u32x2{b1 & mask0, b1 & mask1}; bp[2] = u32x2{b2 & mask0, b2 & mask1}; bp[3] = u32x2{b3 & mask0, b3 & mask1};
    PIN4(bp[0], bp[1], bp[2], bp[3]);
  };
#define VLD(j, i) WAITP(ring[j][i]); o1 = mfma16(u32x2{ring[j][i][0], ring[j][i][1]}, bp[i], o1); o2 = mfma16(u32x2{ring[j][i][2], ring[j][i][3]}, bp[i], o2); LD1(ring[j][i], off, rs, i);
#pragma unroll 1
  for (int t = n - nk; t < n; t += 2) {
    probs(t < 0 ? n : t);
    R4(VLD, 0)
    off += 16384;
    probs(t + 1);
    R4(VLD, 1)
    off += 16384;
  }
  STAMP(2);
  l = x16_32_sum(l);
  LAS float* mg = (LAS float*)(L + L_MRG) + lane;
#pragma unroll
  for (int i = 0; i < 4; ++i) { mg[w * 640 + i * 64] = o1[i]; mg[w * 640 + (4 + i) * 64] = o2[i]; }
  mg[w * 640 + 512] = mL; mg[w * 640 + 576] = l;
  WAIT_LDS();
  if (lds_ticket(ctl_addr + C_MRG * 4) == 3) {
    const unsigned tag = ctl[C_TAG], tb = tag & 0xffu;
    const __amdgpu_buffer_rsrc_t rsw = __builtin_amdgcn_make_buffer_rsrc((void*)p.ws, 0, WS_SPAN, RSRC3);
    float mv[4][10];
#pragma unroll
    for (int u = 0; u < 4; ++u)
#pragma unroll
      for (int i = 0; i < 10; ++i) mv[u][i] = mg[u * 640 + i * 64];
    __builtin_amdgcn_sched_barrier(0x2 | 0x4);
    const float M = fmaxf(fmaxf(mv[0][8], mv[1][8]), fmaxf(mv[2][8], mv[3][8]));
    float Lt = 0.f, acc[8];
#pragma unroll
    for (int i = 0; i < 8; ++i) acc[i] = 0.f;
#pragma unroll
    for (int u = 0; u < 4; ++u) {
      const float f = __builtin_amdgcn_exp2f(mv[u][8] - M);
      Lt = fmaf(f, mv[u][9], Lt);
#pragma unroll
      for (int i = 0; i < 8; ++i) acc[i] = fmaf(f, mv[u][i], acc[i]);
    }
    const float inv = __builtin_amdgcn_rcpf(Lt);
    const int rec = bat * 16 + mem, h = c >> 2;
    const int ro = WS_REC + (kvh * 32 + rec) * 2048 + h * 512 + (r * 4 + (c & 3)) * 16;
#pragma unroll
    for (int i = 0; i < 2; ++i)
      __builtin_amdgcn_raw_buffer_store_b128(u32x4{tagw(acc[2 * i] * inv, tb), tagw(acc[4 + 2 * i] * inv, tb), tagw(acc[2 * i + 1] * inv, tb), tagw(acc[5 + 2 * i] * inv, tb)}, rsw, ro + i * 256, 0, AUX_SC1);
    if (r == 0 && (c & 3) == 0)
      __builtin_amdgcn_raw_buffer_store_b64(u32x2{__float_as_uint(M + __builtin_amdgcn_logf(Lt)), tag}, rsw, WS_HDR + (kvh * 32 + rec) * 128 + h * 8, 0, AUX_SC1);
    MEMBAR();
    ctl[C_PUB] = 1u;
    STAMP(3);
  }
  WAIT4(ring[0], 0); WAIT4(ring[1], 0);
  EXIT();
}

extern "C" void kernel_launch(void* const* d_in, const int* in_sizes, int n_in, void* d_out, int out_size, void* d_ws, size_t ws_size, hipStream_t stream) {
  Params p;
  p.x = (const u16*)d_in[0]; p.kc = (const u16*)d_in[1]; p.vc = (const u16*)d_in[2]; p.wq = (const u16*)d_in[3]; p.wo = (const u16*)d_in[4];
  p.wg = (const u16*)d_in[6]; p.wu = (const u16*)d_in[7]; p.wd = (const u16*)d_in[8];
  p.out = (u16*)d_out; p.ws = (unsigned char*)d_ws;
  for (int grp = 0; grp < 16; ++grp) {
    int cost[16], sum = 0;
    for (int m = 0; m < 16; ++m) {
      const int jb = (m + grp) & 3, ji = m >> 2, bat = grp >> 3;
      int c;
      if (jb == 1) c = C1 + (bat == 0 && ji == 3 ? CS1 : 0);
      else if (jb == 2) c = ((grp & 3) == 3 && (ji & 1)) ? 0 : C2 + (((grp * 2 + (ji & 1) - (grp >> 2)) & 3) == 3 ? CS2 : 0);
      else c = CQ + (jb == 3 && bat == 0 && (ji & 1) ? CSQ : 0);
      c += (m & 1) ? PAR : -(PAR);
      cost[m] = c; sum += c;
    }
    const int tot = 2048 + sum, lvl = tot / 16;
    int rem = tot - lvl * 16, first = 0;
    for (int m = 0; m < 16; ++m) {
      int n = lvl - cost[m];
      if (rem > 0 && (((m * 7) & 15) < tot - lvl * 16)) { ++n; --rem; }
      p.share[grp * 16 + m] = (unsigned)first << 16 | (unsigned)n;
      first += n;
    }
  }
  (void)hipFuncSetAttribute((const void*)fused_decode, hipFuncAttributeMaxDynamicSharedMemorySize, L_BYTES);
  hipLaunchKernelGGL(fused_decode, dim3(NWG), dim3(NTHR), L_BYTES, stream, p);
}
```

```cpp
#include <hip/hip_runtime.h>
#include <stdint.h>

#define STAMPS 0
#define PACE 5
#define PLO 3
#define PHI 7
#define TAU 880
#define TBIAS 0
#define QDELAY 400
#define CODEW 32
#define C1 7
#define C2 15
#define CQ 5
#define CS1 3
#define CS2 4
#define CSQ 3
#define PAR 1

typedef unsigned short u16;
typedef unsigned long long u64;
typedef unsigned u32x4 __attribute__((ext_vector_type(4)));
typedef unsigned u32x2 __attribute__((ext_vector_type(2)));
typedef float f32x4 __attribute__((ext_vector_type(4)));
typedef __bf16 bf16x8_t __attribute__((ext_vector_type(8)));
typedef __bf16 bf16x2_t __attribute__((ext_vector_type(2)));
typedef float f32x2 __attribute__((ext_vector_type(2)));
typedef short s16x4 __attribute__((ext_vector_type(4)));
#define GAS __attribute__((address_space(1)))
#define LAS __attribute__((address_space(3)))

constexpr int DM = 512, SK = 32768, FF = 1792;
constexpr int NWG = 256, NTHR = 512;
constexpr int XW = 10;
constexpr int AUX_NT = 2, AUX_SC1 = 16;
constexpr int RSRC3 = 0x00020000;
constexpr unsigned SPIN_CAP = 1u << 18;

constexpr int WS_EPOCH = 0;
constexpr int WS_Q = 8192;
constexpr int WS_HDR = WS_Q + 32768;
constexpr int WS_REC = WS_HDR + 32768;
constexpr int WS_HP = WS_REC + 262144;
constexpr int WS_DP = WS_HP + 32768;
constexpr int WS_SPAN = 1 << 20;
constexpr int WS_STAMP = 4 << 20;
static_assert(WS_DP + 56 * 4096 <= WS_SPAN, "ws map");

constexpr int ASTR = 1056;
constexpr int L_SLAB = 0;
constexpr int L_Q = 98304;
constexpr int L_A = L_Q + 1024;
constexpr int L_ACT = L_A + 2 * ASTR;
constexpr int L_MRG = L_ACT + 128;
constexpr int L_ST = L_MRG + 4 * 2048;
constexpr int L_RED = L_ST + 128;
constexpr int L_CNT = L_RED + 128;
constexpr int L_SINK = L_CNT + 64;
constexpr int L_SC = L_SINK + 256;
constexpr int L_BYTES = L_SC + 4 * 40 * 128;
constexpr int C_QRDY = 0, C_TAG = 1, C_MRG = 2, C_HS = 3, C_GO = 4, C_WIN = 5, C_PUB = 6, C_KD = 7, C_LSE = 8, C_END = 9;
constexpr int CODE_BYTES = 22528;

struct Params {
  const u16 *x, *kc, *vc, *wq, *wo, *wg, *wu, *wd;
  u16* out;
  unsigned char* ws;
  unsigned share[NWG];
};

__device__ __forceinline__ float bf_lo(unsigned u) { return __builtin_bit_cast(float, u << 16); }
__device__ __forceinline__ float bf_hi(unsigned u) { return __builtin_bit_cast(float, u & 0xffff0000u); }
__device__ __forceinline__ unsigned pk_bf(float lo, float hi) {
  bf16x2_t r = __builtin_convertvector(f32x2{lo, hi}, bf16x2_t);
  return __builtin_bit_cast(unsigned, r);
}
__device__ __forceinline__ float rbf(float f) { return bf_lo(pk_bf(f, f)); }
template <int DC> __device__ __forceinline__ float dpp(float x) {
  return __builtin_bit_cast(float, __builtin_amdgcn_mov_dpp(__builtin_bit_cast(int, x), DC, 0xf, 0xf, true));
}
constexpr int XOR1 = 0xB1, XOR2 = 0x4E, HMIR = 0x141, MIR = 0x140, ROR8 = 0x128, ROR4 = 0x124;
__device__ __forceinline__ float x16_32_max(float x) {
  auto s = __builtin_amdgcn_permlane16_swap(__float_as_uint(x), __float_as_uint(x), false, false);
  x = fmaxf(__uint_as_float(s[0]), __uint_as_float(s[1]));
  auto t = __builtin_amdgcn_permlane32_swap(__float_as_uint(x), __float_as_uint(x), false, false);
  return fmaxf(__uint_as_float(t[0]), __uint_as_float(t[1]));
}
__device__ __forceinline__ float add16(float a, float b) {
  auto s = __builtin_amdgcn_permlane16_swap(__float_as_uint(a), __float_as_uint(b), false, false);
  return __uint_as_float(s[0]) + __uint_as_float(s[1]);
}
__device__ __forceinline__ float add32(float a, float b) {
  auto s = __builtin_amdgcn_permlane32_swap(__float_as_uint(a), __float_as_uint(b), false, false);
  return __uint_as_float(s[0]) + __uint_as_float(s[1]);
}
__device__ __forceinline__ float x16_32_sum(float x) { x = add16(x, x); return add32(x, x); }
__device__ __forceinline__ float wave_sum(float v) { v += dpp<XOR1>(v); v += dpp<XOR2>(v); v += dpp<HMIR>(v); v += dpp<MIR>(v); return x16_32_sum(v); }
__device__ __forceinline__ f32x4 mfma32(u32x4 a, u32x4 b, f32x4 c) {
  return __builtin_amdgcn_mfma_f32_16x16x32_bf16(__builtin_bit_cast(bf16x8_t, a), __builtin_bit_cast(bf16x8_t, b), c, 0, 0, 0);
}
__device__ __forceinline__ f32x4 mfma16(u32x2 a, u32x2 b, f32x4 c) {
  return __builtin_amdgcn_mfma_f32_16x16x16bf16_1k(__builtin_bit_cast(s16x4, a), __builtin_bit_cast(s16x4, b), c, 0, 0, 0);
}
__device__ __forceinline__ unsigned tagw(float v, unsigned hi, unsigned tb) { return (__float_as_uint(v) & hi) | tb; }
__device__ __forceinline__ unsigned or4(u32x4 x) { return (x[0] | x[1]) | (x[2] | x[3]); }
__device__ __forceinline__ unsigned max4(u32x4 x) { return max(max(x[0], x[1]), max(x[2], x[3])); }
#define ZERO4 f32x4{0.f, 0.f, 0.f, 0.f}
#define F4(x) __builtin_bit_cast(f32x4, x)
#define MEMBAR() asm volatile("" ::: "memory")
#define WAIT_LDS() asm volatile("s_waitcnt lgkmcnt(0)" ::: "memory")
#define WAIT_VM() asm volatile("s_waitcnt vmcnt(0)" ::: "memory")
#define BARRIER() do { WAIT_LDS(); __builtin_amdgcn_s_barrier(); MEMBAR(); } while (0)
#define PIN1(a) asm volatile("" : "+v"(a))
#define PIN4(a, b, c, d) asm volatile("" : "+v"(a), "+v"(b), "+v"(c), "+v"(d))
#define NOW() __builtin_amdgcn_s_memrealtime()

#define R4(M, a) M(a, 0) M(a, 1) M(a, 2) M(a, 3)
#define R10(M) M(0) M(1) M(2) M(3) M(4) M(5) M(6) M(7) M(8) M(9)
#define STR2(x) #x
#define STR(x) STR2(x)
#define LD1X(C, d, off, rs) asm volatile("buffer_load_dwordx4 %0, %1, %2, 0 offen nt" : C(d) : "v"(off), "s"(rs))
#define LD1(d, off, rs, i) LD1X("+v", d, (off) + (i) * 4096, rs)
#define LD1I(d, off, rs, i) LD1X("=&v", d, (off) + (i) * 4096, rs)
#define WAITN(n) asm volatile("s_waitcnt vmcnt(" STR(n) ")")
#define WAITD(d, n) asm volatile("s_waitcnt vmcnt(" STR(n) ")" : "+v"(d))
static_assert(PACE <= 7 && PHI <= 7, "ring of eight loads");
__device__ __forceinline__ u32x4 make_srd(const void* base, unsigned bytes) {
  const u64 a = (u64)base;
  return u32x4{(unsigned)a, (unsigned)(a >> 32) & 0xffffu, bytes, (unsigned)RSRC3};
}

#define STAMP(i) do { } while (0)
#define DUMP() do { } while (0)
#define EXIT() do { DUMP(); __builtin_amdgcn_endpgm(); } while (0)

#define DMA16(src, dst) __builtin_amdgcn_global_load_lds((const GAS unsigned*)(src), (LAS unsigned*)(dst), 16, 0, AUX_NT)
__device__ __forceinline__ u32x4 tr_frag(LAS unsigned char* a, int plus4) {
  const u32x2 lo = __builtin_bit_cast(u32x2, __builtin_amdgcn_ds_read_tr16_b64_v4i16((LAS s16x4*)a));
  const u32x2 hi = __builtin_bit_cast(u32x2, __builtin_amdgcn_ds_read_tr16_b64_v4i16((LAS s16x4*)(a + plus4)));
  return u32x4{lo[0], lo[1], hi[0], hi[1]};
}
__device__ __forceinline__ float gemv16(u32x4 a0, u32x4 a1, int lane, const u32x4 (&bf)[16]) {
  f32x4 d[8];
#pragma unroll
  for (int s = 0; s < 8; ++s) d[s] = mfma32(a0, bf[s], ZERO4);
#pragma unroll
  for (int s = 0; s < 8; ++s) d[s] = mfma32(a1, bf[8 + s], d[s]);
  float v[2];
#pragma unroll
  for (int b = 0; b < 2; ++b) {
    const float t0 = d[0][b] + d[1][2 + b], t1 = d[2][b] + d[3][2 + b], t2 = d[4][b] + d[5][2 + b], t3 = d[6][b] + d[7][2 + b];
    const float lo = (lane & 16) ? t1 : t0, hi = (lane & 16) ? t3 : t2;
    v[b] = (lane & 32) ? hi : lo;
  }
  const float x = add16(v[0], v[1]);
  return add32(x, x);
}
__device__ __forceinline__ void lds_wait_ge(volatile LAS unsigned* w, unsigned v) {
#pragma unroll 1
  for (unsigned sp = 0; sp < (SPIN_CAP << 4); ++sp) { if ((unsigned)__builtin_amdgcn_readfirstlane(*w) >= v) break; __builtin_amdgcn_s_sleep(1); }
  MEMBAR();
}
__device__ __forceinline__ void wait_until(u64 t0, unsigned ticks) {
#pragma unroll 1
  for (unsigned sp = 0; sp < 8192u && NOW() - t0 < ticks; ++sp) __builtin_amdgcn_s_sleep(2);
}
__device__ __forceinline__ void lds_count(unsigned addr) {
  u64 keep;
  asm volatile("s_mov_b64 %0, exec\n\ts_mov_b64 exec, 1\n\tds_add_u32 %1, %2\n\ts_mov_b64 exec, %0" : "=&s"(keep) : "v"(addr), "v"(1u) : "memory");
}

__global__ void __launch_bounds__(NTHR, 2) fused_decode(Params p) {
  u64 pc0;
  asm volatile("s_getpc_b64 %0" : "=s"(pc0));
  extern __shared__ __attribute__((aligned(16))) unsigned char smem[];
  LAS unsigned char* L = (LAS unsigned char*)smem;
  volatile LAS unsigned* ctl = (volatile LAS unsigned*)(L + L_CNT);
  const unsigned ctl_addr = (unsigned)(size_t)(L + L_CNT);
  const int g = blockIdx.x, tid = threadIdx.x, lane = tid & 63, w = __builtin_amdgcn_readfirstlane(tid >> 6);
  const int grp = g >> 4, mem = g & 15;
  const int kvh = grp & 7, bat = grp >> 3;
  const int jb = (mem + grp) & 3, ji = mem >> 2;
  const bool hasq = jb == 0 || jb == 3;
  const int qt = ((bat * 2 + (jb == 3)) * 2 + (ji & 1)) * 2 + (ji >> 1);
  const float LOG2E = 1.4426950408889634f;
  if (w == 5 && g < CODEW) {
    unsigned keep;
    const int o0 = lane * 128, o1 = o0 + 8192, o2 = min(o0 + 16384, CODE_BYTES - 128);
    asm volatile("s_mov_b32 %0, m0\n\ts_mov_b32 m0, %1\n\ts_nop 0\n\tglobal_load_lds_dword %2, %5\n\tglobal_load_lds_dword %3, %5\n\tglobal_load_lds_dword %4, %5\n\ts_mov_b32 m0, %0"
                 : "=&s"(keep) : "s"((unsigned)(size_t)(L + L_SINK)), "v"(o0), "v"(o1), "v"(o2), "s"(pc0 & ~(u64)255) : "memory");
  }
  if (w == 4) {
    unsigned keep;
    asm volatile("s_mov_b32 %0, m0\n\ts_mov_b32 m0, %1\n\ts_nop 0\n\tglobal_load_lds_dwordx4 %2, %3\n\ts_add_u32 m0, m0, 0x400\n\ts_nop 0\n\tglobal_load_lds_dwordx4 %4, %3\n\t"
                 "s_add_u32 m0, m0, 0x400\n\ts_nop 0\n\tglobal_load_lds_dword %5, %6 sc1\n\ts_mov_b32 m0, %0"
                 : "=&s"(keep) : "s"((unsigned)(size_t)(L + L_MRG)), "v"(lane * 16), "s"(p.x), "v"(lane * 16 + 1024), "v"(0), "s"(p.ws) : "memory", "scc");
  }

  if (__builtin_expect(w >= 4, 0)) {
    __builtin_amdgcn_s_setprio(3);
    const u64 t_start = NOW();
    const int u = w - 4;
    const int sub = bat * 4 + ji;
    const int job = jb == 2 && bat == 0 && ji == 3 ? 4 : jb;
    const int fj = (bat ? 4 * (grp & 3) + ji : 16 + 3 * (grp & 3) + ji) * 2 + ((grp >> 2) & 1);
    const int rho = sub * 8 + kvh;
    LAS unsigned char* slab = L + L_SLAB;
    if (u == 0) { if (lane < 16) ctl[lane] = 0u; WAIT_LDS(); }
    __builtin_amdgcn_s_barrier();
    if ((hasq && u > 1) || (job == 4 && u != 0)) { WAIT_VM(); EXIT(); }
    const __amdgpu_buffer_rsrc_t rsw = __builtin_amdgcn_make_buffer_rsrc((void*)p.ws, 0, WS_SPAN, RSRC3);
    u32x4 bf[16], bd[8];
    LAS unsigned char* aread = L + L_A + (lane & 1) * ASTR + ((lane >> 1) & 7) * 64 + (lane >> 4) * 16;
    unsigned tag = 0;
    if (u == 0) {
      WAIT_VM();
      const unsigned ep = *(LAS unsigned*)(L + L_MRG + 2048);
      if (hasq) {
#pragma unroll
        for (int b = 0; b < 2; ++b) {
          const u32x4 xv = *(LAS u32x4*)(L + L_MRG + b * 1024 + lane * 16);
          float v[8];
#pragma unroll
          for (int i = 0; i < 4; ++i) { v[2 * i] = bf_lo(xv[i]); v[2 * i + 1] = bf_hi(xv[i]); }
          float s = 0.f;
#pragma unroll
          for (int i = 0; i < 8; ++i) s += v[i];
          const float mean = wave_sum(s) * (1.0f / DM);
          float q2 = 0.f;
#pragma unroll
          for (int i = 0; i < 8; ++i) q2 = fmaf(v[i] - mean, v[i] - mean, q2);
          const float var = wave_sum(q2) * (1.0f / DM);
          const float mb = rbf(mean), rd = __builtin_amdgcn_rcpf(rbf(__builtin_amdgcn_sqrtf(rbf(rbf(var) + 1e-5f))));
          u32x4 o;
#pragma unroll
          for (int i = 0; i < 4; ++i) o[i] = pk_bf(rbf(v[2 * i] - mb) * rd, rbf(v[2 * i + 1] - mb) * rd);
          *(LAS u32x4*)(L + L_A + b * ASTR + lane * 16) = o;
        }
      }
      tag = __builtin_amdgcn_readfirstlane(ep) + 1u;
      ctl[C_TAG] = tag;
      if (hasq) lds_count(ctl_addr + C_HS * 4);
    }
    if (hasq) {
      lds_wait_ge(ctl + C_HS, 5u);
      if (u == 0) STAMP(12);
      tag = __builtin_amdgcn_readfirstlane(ctl[C_TAG]);
      LAS unsigned char* fq = slab + ((lane >> 4) * 8 + ((lane >> 2) & 3)) * 64 + (((lane >> 4) & 1) ^ u) * 32 + (lane & 3) * 8;
#pragma unroll
      for (int s = 0; s < 16; ++s) bf[s] = tr_frag(fq + s * 2048, 256);
      const float v = gemv16(*(LAS u32x4*)aread, *(LAS u32x4*)(aread + 512), lane, bf);
      const float nb = dpp<XOR1>(v);
      if (lane < 32 && !(lane & 1))
        __builtin_amdgcn_raw_buffer_store_b64(u32x2{pk_bf(v, nb), tag}, rsw, WS_Q + ((lane >> 4) * 2048 + kvh * 256 + qt * 16 + u * 8 + ((lane & 15) >> 1)) * 8, 0, AUX_SC1);
      if (u != 0) { WAIT_VM(); EXIT(); }
      STAMP(4);
    }
    if (u == 0) {
      const int qo = WS_Q + (bat * 2048 + kvh * 256 + lane * 2) * 8;
      u32x4 g0, g1;
      wait_until(t_start, QDELAY);
#pragma unroll 1
      for (unsigned sp = 0; sp < SPIN_CAP; ++sp) {
        MEMBAR();
        g0 = __builtin_amdgcn_raw_buffer_load_b128(rsw, qo, 0, AUX_SC1);
        g1 = __builtin_amdgcn_raw_buffer_load_b128(rsw, qo + 1024, 0, AUX_SC1);
        if (__all(g0[1] == tag && g0[3] == tag && g1[1] == tag && g1[3] == tag)) break;
        __builtin_amdgcn_s_sleep(8);
      }
      *(LAS u32x2*)(L + L_Q + lane * 8) = u32x2{g0[0], g0[2]};
      *(LAS u32x2*)(L + L_Q + 512 + lane * 8) = u32x2{g1[0], g1[2]};
      MEMBAR();
      ctl[C_QRDY] = 1u;
      STAMP(5);
    } else lds_wait_ge(ctl + C_WIN, 1u);
    float cs = 0.f;
    u32x2 xr = {0u, 0u};
    const int ocol = (rho >> 3) * 64 + ((((lane >> 4) & 1) << 1) | (lane >> 5)) * 16 + (rho & 3) * 4 + (lane & 3);
    if (job == 1) {
      const unsigned char* src = (const unsigned char*)p.wo + (size_t)(kvh * 512 + u * 128 + 4 * (lane >> 3)) * 1024 + sub * 128 + (lane & 7) * 16;
#pragma unroll
      for (int i = 0; i < 16; ++i) DMA16(src + (32 * (i >> 2) + (i & 3)) * 1024, slab + (u * 16 + i) * 1024);
    } else if (job == 2) {
      xr = *(const GAS u32x2*)((const GAS unsigned char*)p.x + ((lane >> 2) & 1) * 1024 + (u * 128 + (lane >> 3) * 16 + (lane & 3) * 4) * 2);
      const unsigned char* src = (const unsigned char*)((lane & 4) ? p.wu : p.wg) + (size_t)(u * 128 + (lane >> 3)) * (FF * 2) + fj * 64 + (lane & 3) * 16;
#pragma unroll
      for (int i = 0; i < 16; ++i) DMA16(src + (size_t)i * 8 * FF * 2, slab + (u * 16 + i) * 1024);
      const unsigned char* srd = (const unsigned char*)p.wd + (size_t)(fj * 32 + u * 8) * 1024 + lane * 16;
#pragma unroll
      for (int i = 0; i < 8; ++i) DMA16(srd + i * 1024, slab + 65536 + (u * 8 + i) * 1024);
    } else if (job == 3) {
      xr[0] = *(const GAS u16*)((const GAS unsigned char*)p.x + ((rho >> 2) & 1) * 1024 + ocol * 2);
    }
    if (job == 1 || job == 2) {
      WAIT_VM();
      lds_count(ctl_addr + C_HS * 4);
      lds_wait_ge(ctl + C_HS, 4u);
      tag = __builtin_amdgcn_readfirstlane(ctl[C_TAG]);
      LAS unsigned char* fa = slab + ((lane >> 4) * 8 + ((lane >> 2) & 3)) * 128 + (job == 1 ? u * 32 + (lane & 3) * 8 : (lane & 2) * 32 + u * 16 + (lane & 1) * 8);
#pragma unroll
      for (int s = 0; s < 16; ++s) bf[s] = tr_frag(fa + s * 4096, 512);
      if (job == 2) {
        LAS unsigned char* fd = slab + 65536 + ((lane >> 4) * 8 + ((lane >> 2) & 3)) * 1024 + u * 256 + (lane & 3) * 8;
#pragma unroll
        for (int t = 0; t < 8; ++t) bd[t] = tr_frag(fd + t * 32, 4096);
        f32x4 d = ZERO4;
#pragma unroll
        for (int s = 0; s < 16; ++s) d = mfma32(u32x4{0x3f803f80u, 0x3f803f80u, 0x3f803f80u, 0x3f803f80u}, bf[s], d);
        cs = d[0];
      }
    }
    if (u == 0) {
      lds_wait_ge(ctl + C_KD, 4u);
      const int ns = p.share[g] & 0xffff;
      const u64 t_k = NOW();
      const unsigned fin = (unsigned)t_k + ns * TAU / 64;
      const u32x4 ones = {~0u, ~0u, ~0u, ~0u};
      __builtin_amdgcn_raw_buffer_store_b128(ones, rsw, WS_REC + (kvh * 32 + bat * 16 + mem) * 1024 + lane * 16, 0, AUX_SC1);
      __builtin_amdgcn_s_setprio(0);
      const LAS float* smx = (const LAS float*)(L + L_ST) + (lane & 3);
      const float M = fmaxf(fmaxf(smx[0], smx[4]), fmaxf(smx[8], smx[12]));
      const LAS unsigned char* sp0 = L + L_SC + (lane >> 4) * (40 * 128) + (lane & 15) * 8;
      float ls = 0.f;
#pragma unroll 2
      for (int t = 0; t < (ns + 3) >> 2; ++t) {
        const u32x2 sv = *(const LAS u32x2*)(sp0 + t * 128);
        ls += (__builtin_amdgcn_exp2f(fmaf(bf_lo(sv[0]), LOG2E, -M)) + __builtin_amdgcn_exp2f(fmaf(bf_hi(sv[0]), LOG2E, -M)))
            + (__builtin_amdgcn_exp2f(fmaf(bf_lo(sv[1]), LOG2E, -M)) + __builtin_amdgcn_exp2f(fmaf(bf_hi(sv[1]), LOG2E, -M)));
      }
      ls += dpp<ROR4>(ls); ls += dpp<ROR8>(ls); ls = x16_32_sum(ls);
      __builtin_amdgcn_s_setprio(3);
      WAIT_VM();
      if (lane < 4) __builtin_amdgcn_raw_buffer_store_b128(u32x4{__float_as_uint(M + __builtin_amdgcn_logf(ls)), tag, fin, tag}, rsw, WS_HDR + g * 128 + lane * 16, 0, AUX_SC1);
      u32x4 hd;
#pragma unroll 1
      for (unsigned sp = 0; sp < SPIN_CAP; ++sp) {
        __builtin_amdgcn_s_sleep(24);
        MEMBAR();
        hd = __builtin_amdgcn_raw_buffer_load_b128(rsw, WS_HDR + (grp * 16 + (lane >> 2)) * 128 + (lane & 3) * 16, 0, AUX_SC1);
        if (__all(hd[1] == tag && hd[3] == tag)) break;
      }
      const float v = __uint_as_float(hd[0]);
      float mx = fmaxf(v, dpp<ROR4>(v)); mx = fmaxf(mx, dpp<ROR8>(mx)); mx = x16_32_max(mx);
      float e = __builtin_amdgcn_exp2f(v - mx);
      e += dpp<ROR4>(e); e += dpp<ROR8>(e); e = x16_32_sum(e);
      if (lane < 4) *(LAS float*)(L + L_ST + 64 + lane * 4) = mx + __builtin_amdgcn_logf(e);
      ctl[C_END] = (fin + (unsigned)(int)(wave_sum((float)(int)(hd[2] - fin)) * (1.0f / 64)) + TBIAS) | 1u;
      MEMBAR();
      ctl[C_LSE] = 1u;
      STAMP(15);
      if (job == 0 || job == 4) EXIT();
      if (job == 1) {
#pragma unroll 1
        for (unsigned sp = 0; sp < SPIN_CAP; ++sp) {
          MEMBAR();
          const u32x4 t = __builtin_amdgcn_raw_buffer_load_b128(rsw, WS_HDR + ((grp ^ 8) * 16 + (lane & 15)) * 128, 0, AUX_SC1);
          if (__all(t[1] == tag && t[3] == tag)) break;
          __builtin_amdgcn_s_sleep(32);
        }
      }
      lds_wait_ge(ctl + C_PUB, 1u);
      if (job != 1) {
#pragma unroll 1
        for (unsigned sp = 0; sp < SPIN_CAP; ++sp) {
          MEMBAR();
          const unsigned t = __builtin_amdgcn_raw_buffer_load_b32(rsw, WS_REC + (kvh * 32 + bat * 16 + (lane & 15)) * 1024, 0, AUX_SC1);
          if (__all(t != ~0u)) break;
          __builtin_amdgcn_s_sleep(6);
        }
      }
      ctl[C_GO] = 1u;
    }
    const unsigned tb = tag & 0xffu;
    unsigned him = 0xffffff00u;
    PIN1(him);
    if (job == 1) {
      int ro = WS_REC + kvh * 32768 + (lane >> 5) * 16384 + ((lane >> 4) & 1) * 1024 + u * 256 + (lane & 15) * 16;
      int ho = WS_HP + (kvh * 32 + sub * 4 + u) * 128 + (lane & 31) * 4;
      unsigned aw = (unsigned)(size_t)(L + L_A) + (lane >> 5) * ASTR + u * 256 + (lane & 15) * 16 + ((lane >> 4) & 1) * 8;
      const unsigned one = ((lane >> 3) & 1) == (lane >> 5) ? 0x3f80u << (16 * (lane & 1)) : 0u;
      u32x4 sel = {(lane & 6) == 0 ? one : 0u, (lane & 6) == 2 ? one : 0u, (lane & 6) == 4 ? one : 0u, (lane & 6) == 6 ? one : 0u};
      PIN4(ro, ho, aw, sel);
      if (u != 0) lds_wait_ge(ctl + C_GO, 1u);
      f32x4 s0, s1;
#pragma unroll 1
      for (unsigned sp = 0; sp < SPIN_CAP; ++sp) {
        MEMBAR();
        u32x4 gr[8];
#pragma unroll
        for (int q = 0; q < 8; ++q) gr[q] = __builtin_amdgcn_raw_buffer_load_b128(rsw, ro, q * 2048, AUX_SC1);
        __builtin_amdgcn_sched_barrier(0);
        unsigned mx = 0;
        s0 = ZERO4; s1 = ZERO4;
#pragma unroll
        for (int q = 0; q < 8; q += 2) {
          s0 = mfma32(sel, gr[q], s0); s1 = mfma32(sel, gr[q + 1], s1);
          mx = max(mx, max(max4(gr[q]), max4(gr[q + 1])));
        }
        if (__all(mx != ~0u)) break;
      }
      if (u == 0) STAMP(6);
      s0 += s1;
      *(LAS u32x2*)(size_t)aw = u32x2{pk_bf(s0[0], s0[1]), pk_bf(s0[2], s0[3])};
      BARRIER();
      if (u == 0) STAMP(13);
      const float v = gemv16(*(LAS u32x4*)aread, *(LAS u32x4*)(aread + 512), lane, bf);
      __builtin_amdgcn_raw_buffer_store_b32(tagw(v, him, tb), rsw, ho, 0, AUX_SC1);
      if (u == 0) STAMP(7);
    } else if (job == 2) {
      int po = WS_HP + u * 1024 + lane * 16;
      int dpo = WS_DP + fj * 4096 + u * 1024 + lane * 16;
      unsigned hw = (unsigned)(size_t)(L + L_A) + ((lane >> 2) & 1) * ASTR + (u * 128 + (lane >> 3) * 16 + (lane & 3) * 4) * 2;
      unsigned sw = (unsigned)(size_t)(L + L_RED) + ((lane >> 2) & 1) * 64 + u * 16 + (lane >> 5) * 8;
      unsigned sr = (unsigned)(size_t)(L + L_RED) + ((lane >> 4) & 1) * 64;
      unsigned cw = (lane < 32 && (lane & 9) == 0) ? (unsigned)(size_t)(L + L_ACT) + (lane >> 4) * 64 + u * 16 + (lane & 7) * 2 : (unsigned)(size_t)(L + L_SINK) + lane * 4;
      unsigned cr = (unsigned)(size_t)(L + L_ACT) + ((lane >> 2) & 1) * 64 + (lane >> 4) * 16;
      f32x4 xf = {bf_lo(xr[0]), bf_hi(xr[0]), bf_lo(xr[1]), bf_hi(xr[1])};
      PIN4(po, dpo, hw, sw); PIN4(sr, cw, cr, xf);
      if (u != 0) lds_wait_ge(ctl + C_GO, 1u);
      u32x4 gp[8];
#pragma unroll 1
      for (unsigned sp = 0; sp < SPIN_CAP; ++sp) {
        MEMBAR();
#pragma unroll
        for (int q = 0; q < 8; ++q) gp[q] = __builtin_amdgcn_raw_buffer_load_b128(rsw, po, q * 4096, AUX_SC1);
        __builtin_amdgcn_sched_barrier(0);
        unsigned bad = 0;
#pragma unroll
        for (int q = 0; q < 8; ++q) { gp[q] ^= tb; bad |= or4(gp[q]); }
        if (__all((bad & 0xffu) == 0u)) break;
      }
      if (u == 0) STAMP(8);
      {
        const f32x4 at = ((F4(gp[0]) + F4(gp[1])) + (F4(gp[2]) + F4(gp[3]))) + ((F4(gp[4]) + F4(gp[5])) + (F4(gp[6]) + F4(gp[7])));
        const unsigned a01 = pk_bf(at[0], at[1]), a23 = pk_bf(at[2], at[3]);
        const unsigned h01 = pk_bf(xf[0] + bf_lo(a01), xf[1] + bf_hi(a01)), h23 = pk_bf(xf[2] + bf_lo(a23), xf[3] + bf_hi(a23));
        *(LAS u32x2*)(size_t)hw = u32x2{h01, h23};
        const unsigned p0 = __builtin_amdgcn_readlane(h01, 0), p4 = __builtin_amdgcn_readlane(h01, 4);
        const float pv = bf_lo((lane & 4) ? p4 : p0);
        const float d0 = bf_lo(h01) - pv, d1 = bf_hi(h01) - pv, d2 = bf_lo(h23) - pv, d3 = bf_hi(h23) - pv;
        float t = add32((d0 + d1) + (d2 + d3), fmaf(d0, d0, d1 * d1) + fmaf(d2, d2, d3 * d3));
        t = add16(t, t);
        t += dpp<XOR1>(t); t += dpp<XOR2>(t); t += dpp<ROR8>(t);
        *(LAS f32x2*)(size_t)sw = f32x2{t, pv};
      }
      BARRIER();
      if (u == 0) STAMP(14);
      const u32x4 a0 = *(LAS u32x4*)aread, a1 = *(LAS u32x4*)(aread + 512);
      f32x4 st[4];
#pragma unroll
      for (int k = 0; k < 4; ++k) st[k] = *(LAS f32x4*)(size_t)(sr + k * 16);
      __builtin_amdgcn_sched_barrier(0);
      const float dv = gemv16(a0, a1, lane, bf);
      float mk[4], sk[4];
#pragma unroll
      for (int k = 0; k < 4; ++k) { mk[k] = fmaf(st[k][0], 1.0f / 128, st[k][1]); sk[k] = fmaf(st[k][0] * (-1.0f / 128), st[k][0], st[k][2]); }
      const float mean = 0.25f * ((mk[0] + mk[1]) + (mk[2] + mk[3]));
#pragma unroll
      for (int k = 0; k < 4; ++k) { const float dm = mk[k] - mean; sk[k] = fmaf(128.0f * dm, dm, sk[k]); }
      const float ss = (sk[0] + sk[1]) + (sk[2] + sk[3]);
      const float mb = rbf(mean), rd = __builtin_amdgcn_rcpf(rbf(__builtin_amdgcn_sqrtf(rbf(rbf(fmaxf(ss, 0.f) * (1.0f / DM)) + 1e-5f))));
#pragma unroll
      for (int k = 0; k < 16; ++k) { __builtin_amdgcn_sched_group_barrier(0x8, 1, 0); __builtin_amdgcn_sched_group_barrier(0x402, 3, 0); }
      const float gv = rbf(fmaf(-mb, cs, dv) * rd);
      const float uv = dpp<ROR8>(gv);
      const float sg = rbf(gv * __builtin_amdgcn_rcpf(1.0f + __builtin_amdgcn_exp2f(-gv * LOG2E)));
      const float av = sg * uv;
      *(LAS unsigned*)(size_t)cw = pk_bf(av, dpp<XOR1>(av));
      BARRIER();
      if (u == 0) STAMP(16);
      const u32x4 aa = *(LAS u32x4*)(size_t)cr;
      u32x4 o;
#pragma unroll
      for (int i = 0; i < 4; ++i) {
        const float e0 = mfma32(aa, bd[i], ZERO4)[0], e1 = mfma32(aa, bd[4 + i], ZERO4)[0];
        o[i] = tagw((lane & 32) ? e1 : e0, him, tb);
      }
      __builtin_amdgcn_raw_buffer_store_b128(o, rsw, dpo, 0, AUX_SC1);
      if (u == 0) STAMP(9);
    } else {
      const int hpo = WS_HP + (ocol >> 4) * 128 + ((rho >> 2) & 1) * 64 + (ocol & 15) * 4;
      unsigned gp[8];
#pragma unroll 1
      for (unsigned sp = 0; sp < SPIN_CAP; ++sp) {
        MEMBAR();
        unsigned bad = 0;
#pragma unroll
        for (int s = 0; s < 8; ++s) { gp[s] = __builtin_amdgcn_raw_buffer_load_b32(rsw, hpo, s * 4096, AUX_SC1) ^ tb; bad |= gp[s]; }
        if (__all((bad & 0xffu) == 0u)) break;
        __builtin_amdgcn_s_sleep(4);
      }
#define U2F(x) __uint_as_float(x)
      float hres = rbf(bf_lo(xr[0]) + rbf(((U2F(gp[0]) + U2F(gp[1])) + (U2F(gp[2]) + U2F(gp[3]))) + ((U2F(gp[4]) + U2F(gp[5])) + (U2F(gp[6]) + U2F(gp[7])))));
      int ao = WS_DP + (lane >> 2) * 4096 + rho * 64 + (lane & 3) * 16;
      int ao3 = lane < 32 ? ao + 3 * 65536 : WS_SPAN;
      int oo = (((rho >> 2) & 1) * 512 + ocol) * 2;
      const __amdgpu_buffer_rsrc_t rso = __builtin_amdgcn_make_buffer_rsrc((void*)p.out, 0, 2048, RSRC3);
      unsigned tb3 = lane < 32 ? tb : 0u;
      PIN4(hres, ao, ao3, oo); PIN1(tb3);
      u32x4 ga[4];
#pragma unroll 1
      for (unsigned sp = 0; sp < SPIN_CAP; ++sp) {
        MEMBAR();
#pragma unroll
        for (int i = 0; i < 3; ++i) ga[i] = __builtin_amdgcn_raw_buffer_load_b128(rsw, ao, i * 65536, AUX_SC1) ^ tb;
        ga[3] = __builtin_amdgcn_raw_buffer_load_b128(rsw, ao3, 0, AUX_SC1) ^ tb3;
        if (__all((or4((ga[0] | ga[1]) | (ga[2] | ga[3])) & 0xffu) == 0u)) break;
      }
      STAMP(10);
      const f32x4 y4 = (F4(ga[0]) + F4(ga[1])) + (F4(ga[2]) + F4(ga[3]));
      float y = add16(add32(y4[0], y4[1]), add32(y4[2], y4[3]));
      y += dpp<ROR8>(y); y += dpp<ROR4>(y);
      __builtin_amdgcn_raw_buffer_store_b16((u16)pk_bf(hres + rbf(y), 0.f), rso, oo, 0, AUX_SC1);
      if (rho == 0 && lane == 0) __builtin_amdgcn_raw_buffer_store_b32(tag, rsw, WS_EPOCH, 0, AUX_SC1);
      STAMP(11);
    }
    EXIT();
  }

  if (w == 0) STAMP(0);
  const int r = lane >> 4, c = lane & 15;
  const unsigned sh = p.share[g];
  const int ns = sh & 0xffff;
  const size_t base = (size_t)grp * SK * 256 + (size_t)(sh >> 16) * 4096;
  u32x4 rs = make_srd((const unsigned char*)p.kc + base, ns * 4096);
  const int klane = (c >> 2) * 256 + r * 64 + (c & 3) * 16;
  int off = w * 1024 + klane;
  const int n = (ns + 3) >> 2;
  u32x4 ring[2][4], xk[XW][4];
  if (hasq) {
    const u32x4 rsq = make_srd(p.wq, 512 * 8192);
    const int qo = (lane >> 2) * 8192 + (((lane & 3) ^ ((lane >> 4) & 2)) << 4);
    int so = w * 128 * 8192 + (kvh * 512 + qt * 32) * 2;
    unsigned dst = (unsigned)(size_t)(L + L_SLAB) + w * 8192;
#pragma unroll 1
    for (int i = 0; i < 8; ++i) {
      unsigned keep;
      asm volatile("s_mov_b32 %0, m0\n\ts_mov_b32 m0, %1\n\ts_nop 0\n\tbuffer_load_dwordx4 %2, %3, %4 offen nt lds\n\ts_mov_b32 m0, %0"
                   : "=&s"(keep) : "s"(dst), "v"(qo), "s"(rsq), "s"(so) : "memory");
      so += 16 * 8192; dst += 1024;
    }
  }
  __builtin_amdgcn_sched_barrier(0);
#define FIRST(j, i) LD1I(xk[j][i], off, rs, i);
#define AHEAD1(j, i) WAITN(PACE); LD1I(xk[j][i], off, rs, i);
#define AHEAD(j) if (j > 0) { R4(AHEAD1, j) off += 16384; }
#define RING1(j, i) WAITN(PACE); LD1I(ring[j][i], off, rs, i);
  R4(FIRST, 0)
  off += 16384;
  __builtin_amdgcn_s_barrier();
  if (hasq) { asm volatile("s_waitcnt vmcnt(4)" ::: "memory"); lds_count(ctl_addr + C_HS * 4); }
  R10(AHEAD)
  R4(RING1, 0)
  off += 16384;
  R4(RING1, 1)
  off += 16384;
  __builtin_amdgcn_sched_barrier(0);
  ctl[C_WIN] = 1u;
  if (w == 0) STAMP(18);
  lds_wait_ge(ctl + C_QRDY, 1u);
  const u32x4 qf = *(LAS u32x4*)(L + L_Q + klane);
  WAITN(8);
#define PINX(x) PIN4(xk[x][0], xk[x][1], xk[x][2], xk[x][3]);
  R10(PINX)
  if (w == 0) STAMP(1);
  const float SCALE = 0.08838834764831845f;
  LAS unsigned char* sc = L + L_SC + w * (40 * 128) + (lane >> 2) * 8;
  float mL = -1.0e38f;
  auto scores = [&](const f32x4 (&d)[4], int t) {
    float S[4];
#pragma unroll
    for (int i = 0; i < 4; ++i) {
      float x = (c & 2) ? ((c & 1) ? d[i][3] : d[i][2]) : ((c & 1) ? d[i][1] : d[i][0]);
      x += dpp<XOR1>(x); x += dpp<XOR2>(x);
      S[i] = x;
    }
    const unsigned a01 = pk_bf(S[0], S[1]), a23 = pk_bf(S[2], S[3]);
    unsigned b01 = pk_bf(bf_lo(a01) * SCALE, bf_hi(a01) * SCALE), b23 = pk_bf(bf_lo(a23) * SCALE, bf_hi(a23) * SCALE);
    if (4 * t + 4 > ns) {
      const int rem = ns - 4 * t;
      b01 = rem > 1 ? b01 : (rem > 0 ? (b01 & 0xffffu) | 0xff800000u : 0xff80ff80u);
      b23 = rem > 2 ? (b23 & 0xffffu) | 0xff800000u : 0xff80ff80u;
    }
    mL = fmaxf(fmaxf(mL, fmaxf(bf_lo(b01), bf_hi(b01))), fmaxf(bf_lo(b23), bf_hi(b23)));
    *(LAS u32x2*)(sc + t * 128) = u32x2{b01, b23};
  };
  const int nk = n + (n & 1);
#pragma unroll 1
  for (int step = XW; step < nk; step += 2) {
    if (step + 2 == nk) {
      rs = make_srd((const unsigned char*)p.vc + base, ns * 4096);
      off = w * 1024 + lane * 16 - (nk - n) * 16384;
    }
    f32x4 d[4];
#define KLD(j, i) WAITD(ring[j][i], PACE); d[i] = mfma32(ring[j][i], qf, ZERO4); LD1(ring[j][i], off, rs, i);
#define XMM(x, i) d[i] = mfma32(xk[x][i], qf, ZERO4);
#define XTILE(j, y) if (step == XW + 2 * y) { R4(XMM, 2 * y + j) scores(d, 2 * y + j); }
#define KSTEP(j) R4(KLD, j) off += 16384; scores(d, step + j); if (step < 2 * XW) { XTILE(j, 0) XTILE(j, 1) XTILE(j, 2) XTILE(j, 3) XTILE(j, 4) }
    KSTEP(0)
    KSTEP(1)
  }
  mL = x16_32_max(mL) * LOG2E;
  *(LAS float*)(L + L_ST + w * 16 + (c >> 2) * 4) = mL;
  lds_count(ctl_addr + C_KD * 4);
  if (w == 0) STAMP(17);
  const unsigned mask0 = (c & 3) == 0 ? 0x0000ffffu : ((c & 3) == 1 ? 0xffff0000u : 0u);
  const unsigned mask1 = (c & 3) == 2 ? 0x0000ffffu : ((c & 3) == 3 ? 0xffff0000u : 0u);
  f32x4 o1 = ZERO4, o2 = ZERO4;
  u32x2 bp[4];
  float f = 0.f;
  auto probs = [&](int t) {
    const u32x2 sv = *(LAS u32x2*)(sc + t * 128);
    const float p0 = __builtin_amdgcn_exp2f(fmaf(bf_lo(sv[0]), LOG2E, -mL)), p1 = __builtin_amdgcn_exp2f(fmaf(bf_hi(sv[0]), LOG2E, -mL));
    const float p2 = __builtin_amdgcn_exp2f(fmaf(bf_lo(sv[1]), LOG2E, -mL)), p3 = __builtin_amdgcn_exp2f(fmaf(bf_hi(sv[1]), LOG2E, -mL));
    const unsigned b0 = pk_bf(p0, p0), b1 = pk_bf(p1, p1), b2 = pk_bf(p2, p2), b3 = pk_bf(p3, p3);
    bp[0] = u32x2{b0 & mask0, b0 & mask1}; bp[1] = u32x2{b1 & mask0, b1 & mask1}; bp[2] = u32x2{b2 & mask0, b2 & mask1}; bp[3] = u32x2{b3 & mask0, b3 & mask1};
    PIN4(bp[0], bp[1], bp[2], bp[3]);
  };
#define VLD(j, i) if (ahead) WAITN(PLO); WAITD(ring[j][i], PHI); \
    o1 = mfma16(u32x2{ring[j][i][0], ring[j][i][1]}, bp[i], o1); o2 = mfma16(u32x2{ring[j][i][2], ring[j][i][3]}, bp[i], o2); LD1(ring[j][i], off, rs, i);
#pragma unroll 1
  for (int t = n - nk; t < n; t += 2) {
    bool ahead = (t & 2) != 0;
    const unsigned te = __builtin_amdgcn_readfirstlane(ctl[C_END]);
    if (te) ahead = (int)(te - (unsigned)NOW()) * 64 > (ns - 4 * t) * TAU;
    if (t + 2 >= n) {
      lds_wait_ge(ctl + C_LSE, 1u);
      f = __builtin_amdgcn_exp2f(mL - *(LAS float*)(L + L_ST + 64 + (c >> 2) * 4));
    }
    probs(t < 0 ? n : t);
    R4(VLD, 0)
    off += 16384;
    probs(t + 1);
    R4(VLD, 1)
    off += 16384;
  }
  STAMP(2);
  LAS f32x4* mg = (LAS f32x4*)(L + L_MRG) + lane;
  mg[w * 128] = f32x4{o1[0] * f, o2[0] * f, o1[1] * f, o2[1] * f};
  mg[w * 128 + 64] = f32x4{o1[2] * f, o2[2] * f, o1[3] * f, o2[3] * f};
  unsigned tk; u64 keep;
  f32x4 m0, m1, m2, m3, m4, m5, m6, m7;
  asm volatile("s_mov_b64 %1, exec\n\ts_mov_b64 exec, 1\n\tds_add_rtn_u32 %0, %10, %11\n\ts_mov_b64 exec, %1\n\t"
               "ds_read_b128 %2, %12\n\tds_read_b128 %3, %12 offset:2048\n\tds_read_b128 %4, %12 offset:4096\n\tds_read_b128 %5, %12 offset:6144\n\t"
               "ds_read_b128 %6, %12 offset:1024\n\tds_read_b128 %7, %12 offset:3072\n\tds_read_b128 %8, %12 offset:5120\n\tds_read_b128 %9, %12 offset:7168\n\ts_waitcnt lgkmcnt(0)"
               : "=&v"(tk), "=&s"(keep), "=&v"(m0), "=&v"(m1), "=&v"(m2), "=&v"(m3), "=&v"(m4), "=&v"(m5), "=&v"(m6), "=&v"(m7)
               : "v"(ctl_addr + C_MRG * 4), "v"(1u), "v"((unsigned)(size_t)mg) : "memory");
  if (__builtin_amdgcn_readfirstlane(tk) == 3) {
    const __amdgpu_buffer_rsrc_t rsw = __builtin_amdgcn_make_buffer_rsrc((void*)p.ws, 0, WS_SPAN, RSRC3);
    const int ro = WS_REC + (kvh * 32 + bat * 16 + mem) * 1024 + (c >> 2) * 256 + (r * 4 + (c & 3)) * 16;
    const f32x4 a = (m0 + m1) + (m2 + m3), b = (m4 + m5) + (m6 + m7);
    __builtin_amdgcn_raw_buffer_store_b128(u32x4{min(pk_bf(a[0], a[1]), ~1u), min(pk_bf(a[2], a[3]), ~1u), min(pk_bf(b[0], b[1]), ~1u), min(pk_bf(b[2], b[3]), ~1u)}, rsw, ro, 0, AUX_SC1);
    MEMBAR();
    ctl[C_PUB] = 1u;
    STAMP(3);
  }
  asm volatile("s_waitcnt vmcnt(0)" : "+v"(ring[0][0]), "+v"(ring[0][1]), "+v"(ring[0][2]), "+v"(ring[0][3]), "+v"(ring[1][0]), "+v"(ring[1][1]), "+v"(ring[1][2]), "+v"(ring[1][3]));
  EXIT();
}

extern "C" void kernel_launch(void* const* d_in, const int* in_sizes, int n_in, void* d_out, int out_size, void* d_ws, size_t ws_size, hipStream_t stream) {
  Params p;
  p.x = (const u16*)d_in[0]; p.kc = (const u16*)d_in[1]; p.vc = (const u16*)d_in[2]; p.wq = (const u16*)d_in[3]; p.wo = (const u16*)d_in[4];
  p.wg = (const u16*)d_in[6]; p.wu = (const u16*)d_in[7]; p.wd = (const u16*)d_in[8];
  p.out = (u16*)d_out; p.ws = (unsigned char*)d_ws;
  for (int grp = 0; grp < 16; ++grp) {
    int cost[16], sum = 0;
    for (int m = 0; m < 16; ++m) {
      const int jb = (m + grp) & 3, ji = m >> 2, bat = grp >> 3;
      int c;
      if (jb == 1) c = C1 + (bat == 0 && ji == 3 ? CS1 : 0);
      else if (jb == 2) c = (bat == 0 && ji == 3) ? 0 : C2 + (((bat ? ji : 3 * (grp & 3) + ji) & 3) == 3 ? CS2 : 0);
      else c = CQ + (jb == 3 && bat == 0 && (ji & 1) ? CSQ : 0);
      c += (m & 1) ? PAR : -(PAR);
      cost[m] = c; sum += c;
    }
    const int tot = 2048 + sum, lvl = tot / 16, rem = tot - lvl * 16;
    int first = 0;
    for (int m = 0; m < 16; ++m) {
      const int n = lvl - cost[m] + (((m * 7) & 15) < rem ? 1 : 0);
      p.share[grp * 16 + m] = (unsigned)first << 16 | (unsigned)n;
      first += n;
    }
  }
  (void)hipFuncSetAttribute((const void*)fused_decode, hipFuncAttributeMaxDynamicSharedMemorySize, L_BYTES);
  hipLaunchKernelGGL(fused_decode, dim3(NWG), dim3(NTHR), L_BYTES, stream, p);
}
```

```cpp
#include <hip/hip_runtime.h>
#include <stdint.h>

#define PACE 5
#define KLO 4
#define PLO 3
#define PHI 7
constexpr int KEND = 2000;
constexpr int TAUK = 900;
constexpr int TAU = 880;
constexpr int QDELAY = 400;
constexpr int FE = 60, FES = 80;
constexpr int FB = 18;
constexpr int NE = 4;
constexpr int RD = 100;

typedef unsigned short u16;
typedef unsigned long long u64;
typedef unsigned u32x4 __attribute__((ext_vector_type(4)));
typedef unsigned u32x2 __attribute__((ext_vector_type(2)));
typedef unsigned long long u64x8 __attribute__((ext_vector_type(8)));
typedef unsigned long long u64x2 __attribute__((ext_vector_type(2)));
typedef float f32x4 __attribute__((ext_vector_type(4)));
typedef __bf16 bf16x8_t __attribute__((ext_vector_type(8)));
typedef __bf16 bf16x2_t __attribute__((ext_vector_type(2)));
typedef float f32x2 __attribute__((ext_vector_type(2)));
typedef short s16x4 __attribute__((ext_vector_type(4)));
#define GAS __attribute__((address_space(1)))
#define LAS __attribute__((address_space(3)))

constexpr int DM = 512, SK = 32768, FF = 1792;
constexpr int NWG = 256, NTHR = 512;
constexpr int NT = 32;
constexpr int XW = 10;
constexpr int AUX_NT = 2, AUX_SC1 = 16;
constexpr int RSRC3 = 0x00020000;
constexpr unsigned SPIN_CAP = 1u << 18;

constexpr int WS_EPOCH = 0;
constexpr int WS_Q = 8192;
constexpr int WS_HDR = WS_Q + 32768;
constexpr int WS_REC = WS_HDR + 32768;
constexpr int WS_HP = WS_REC + 262144;
constexpr int WS_DP = WS_HP + 32768;
constexpr int WS_SPAN = 1 << 20;
static_assert(WS_DP + 56 * 4096 <= WS_SPAN, "ws map");

constexpr int ASTR = 1056;
constexpr int L_SLAB = 0;
constexpr int L_Q = 98304;
constexpr int L_A = L_Q + 1024;
constexpr int L_ACT = L_A + 2 * ASTR;
constexpr int L_MRG = L_ACT + 128;
constexpr int L_ST = L_MRG + 4 * 2048;
constexpr int L_RED = L_ST + 128;
constexpr int L_CNT = L_RED + 128;
constexpr int L_SINK = L_CNT + 64;
constexpr int L_SC = L_SINK + 256;
constexpr int L_BYTES = L_SC + 4 * NT * 128;
constexpr int C_QRDY = 0, C_TAG = 1, C_MRG = 2, C_HS = 3, C_GO = 4, C_PUB = 5, C_KD = 6, C_LSE = 7, C_END = 8;
constexpr int CODE_BYTES = 17920;

struct Params {
  const u16 *kc, *wq, *x;
  unsigned char* ws;
  const u16 *vc, *wo, *wg, *wu, *wd;
  u16* out;
};
static_assert(sizeof(Params) == 80, "ten pointers");

__device__ __forceinline__ float bf_lo(unsigned u) { return __builtin_bit_cast(float, u << 16); }
__device__ __forceinline__ float bf_hi(unsigned u) { return __builtin_bit_cast(float, u & 0xffff0000u); }
__device__ __forceinline__ unsigned pk_bf(float lo, float hi) {
  bf16x2_t r = __builtin_convertvector(f32x2{lo, hi}, bf16x2_t);
  return __builtin_bit_cast(unsigned, r);
}
__device__ __forceinline__ float rbf(float f) { return bf_lo(pk_bf(f, f)); }
template <int DC> __device__ __forceinline__ float dpp(float x) {
  return __builtin_bit_cast(float, __builtin_amdgcn_mov_dpp(__builtin_bit_cast(int, x), DC, 0xf, 0xf, true));
}
template <int DC> __device__ __forceinline__ unsigned dppu(unsigned x) { return (unsigned)__builtin_amdgcn_mov_dpp((int)x, DC, 0xf, 0xf, true); }
constexpr int XOR1 = 0xB1, XOR2 = 0x4E, HMIR = 0x141, MIR = 0x140, ROR8 = 0x128, ROR4 = 0x124;
__device__ __forceinline__ float x16_32_max(float x) {
  auto s = __builtin_amdgcn_permlane16_swap(__float_as_uint(x), __float_as_uint(x), false, false);
  x = fmaxf(__uint_as_float(s[0]), __uint_as_float(s[1]));
  auto t = __builtin_amdgcn_permlane32_swap(__float_as_uint(x), __float_as_uint(x), false, false);
  return fmaxf(__uint_as_float(t[0]), __uint_as_float(t[1]));
}
__device__ __forceinline__ float add16(float a, float b) {
  auto s = __builtin_amdgcn_permlane16_swap(__float_as_uint(a), __float_as_uint(b), false, false);
  return __uint_as_float(s[0]) + __uint_as_float(s[1]);
}
__device__ __forceinline__ float add32(float a, float b) {
  auto s = __builtin_amdgcn_permlane32_swap(__float_as_uint(a), __float_as_uint(b), false, false);
  return __uint_as_float(s[0]) + __uint_as_float(s[1]);
}
__device__ __forceinline__ float x16_32_sum(float x) { x = add16(x, x); return add32(x, x); }
__device__ __forceinline__ float wave_sum(float v) { v += dpp<XOR1>(v); v += dpp<XOR2>(v); v += dpp<HMIR>(v); v += dpp<MIR>(v); return x16_32_sum(v); }
__device__ __forceinline__ f32x4 mfma32(u32x4 a, u32x4 b, f32x4 c) {
  return __builtin_amdgcn_mfma_f32_16x16x32_bf16(__builtin_bit_cast(bf16x8_t, a), __builtin_bit_cast(bf16x8_t, b), c, 0, 0, 0);
}
__device__ __forceinline__ f32x4 mfma16(u32x2 a, u32x2 b, f32x4 c) {
  return __builtin_amdgcn_mfma_f32_16x16x16bf16_1k(__builtin_bit_cast(s16x4, a), __builtin_bit_cast(s16x4, b), c, 0, 0, 0);
}
__device__ __forceinline__ unsigned tagw(float v, unsigned hi, unsigned tb) { return (__float_as_uint(v) & hi) | tb; }
__device__ __forceinline__ unsigned or4(u32x4 x) { return (x[0] | x[1]) | (x[2] | x[3]); }
__device__ __forceinline__ unsigned max4(u32x4 x) { return max(max(x[0], x[1]), max(x[2], x[3])); }
#define ZERO4 f32x4{0.f, 0.f, 0.f, 0.f}
#define F4(x) __builtin_bit_cast(f32x4, x)
#define MEMBAR() asm volatile("" ::: "memory")
#define WAIT_LDS() asm volatile("s_waitcnt lgkmcnt(0)" ::: "memory")
#define WAIT_VM() asm volatile("s_waitcnt vmcnt(0)" ::: "memory")
#define BARRIER() do { WAIT_LDS(); __builtin_amdgcn_s_barrier(); MEMBAR(); } while (0)
#define PIN1(a) asm volatile("" : "+v"(a))
#define PIN4(a, b, c, d) asm volatile("" : "+v"(a), "+v"(b), "+v"(c), "+v"(d))
#define NOW() __builtin_amdgcn_s_memrealtime()

#define R4(M, a) M(a, 0) M(a, 1) M(a, 2) M(a, 3)
#define R10(M) M(0) M(1) M(2) M(3) M(4) M(5) M(6) M(7) M(8) M(9)
#define STR2(x) #x
#define STR(x) STR2(x)
#define LD1X(C, d, off, rs, i) asm volatile("buffer_load_dwordx4 %0, %1, %2, 0 offen offset:1024*" #i " nt" : C(d) : "v"(off), "s"(rs))
#define LD1(d, off, rs, i) LD1X("+v", d, off, rs, i)
#define LD1I(d, off, rs, i) LD1X("=&v", d, off, rs, i)
#define WAITN(n) asm volatile("s_waitcnt vmcnt(" STR(n) ")")
#define WAITD(d, n) asm volatile("s_waitcnt vmcnt(" STR(n) ")" : "+v"(d))
static_assert(PACE <= 7 && PHI <= 7, "ring of eight loads");
__device__ __forceinline__ u32x4 make_srd(const void* base, unsigned bytes) {
  const u64 a = (u64)base;
  return u32x4{(unsigned)a, (unsigned)(a >> 32) & 0xffffu, bytes, (unsigned)RSRC3};
}

#define EXIT() __builtin_amdgcn_endpgm()

#define DMA16(src, dst) __builtin_amdgcn_global_load_lds((const GAS unsigned*)(src), (LAS unsigned*)(dst), 16, 0, AUX_NT)
__device__ __forceinline__ u32x4 tr_frag(LAS unsigned char* a, int plus4) {
  const u32x2 lo = __builtin_bit_cast(u32x2, __builtin_amdgcn_ds_read_tr16_b64_v4i16((LAS s16x4*)a));
  const u32x2 hi = __builtin_bit_cast(u32x2, __builtin_amdgcn_ds_read_tr16_b64_v4i16((LAS s16x4*)(a + plus4)));
  return u32x4{lo[0], lo[1], hi[0], hi[1]};
}
__device__ __forceinline__ void gemv_mm(u32x4 a0, u32x4 a1, const u32x4 (&bf)[16], f32x4 (&d)[8]) {
#pragma unroll
  for (int s = 0; s < 8; ++s) d[s] = mfma32(a0, bf[s], ZERO4);
#pragma unroll
  for (int s = 0; s < 8; ++s) d[s] = mfma32(a1, bf[8 + s], d[s]);
}
__device__ __forceinline__ float gemv_red(const f32x4 (&d)[8], int lane) {
  float v[2];
#pragma unroll
  for (int b = 0; b < 2; ++b) {
    const float t0 = d[0][b] + d[1][2 + b], t1 = d[2][b] + d[3][2 + b], t2 = d[4][b] + d[5][2 + b], t3 = d[6][b] + d[7][2 + b];
    const float lo = (lane & 16) ? t1 : t0, hi = (lane & 16) ? t3 : t2;
    v[b] = (lane & 32) ? hi : lo;
  }
  const float x = add16(v[0], v[1]);
  return add32(x, x);
}
__device__ __forceinline__ float gemv16(u32x4 a0, u32x4 a1, int lane, const u32x4 (&bf)[16]) {
  f32x4 d[8];
  gemv_mm(a0, a1, bf, d);
  __builtin_amdgcn_sched_barrier(0);
  return gemv_red(d, lane);
}
__device__ __forceinline__ void lds_wait_ge(volatile LAS unsigned* w, unsigned v) {
#pragma unroll 1
  for (unsigned sp = 0; sp < (SPIN_CAP << 4); ++sp) { if ((unsigned)__builtin_amdgcn_readfirstlane(*w) >= v) break; __builtin_amdgcn_s_sleep(1); }
  MEMBAR();
}
__device__ __forceinline__ void wait_until(u64 t0, unsigned ticks) {
#pragma unroll 1
  for (unsigned sp = 0; sp < 8192u && NOW() - t0 < ticks; ++sp) __builtin_amdgcn_s_sleep(2);
}
__device__ __forceinline__ void lds_count(unsigned addr) {
  u64 keep;
  asm volatile("s_mov_b64 %0, exec\n\ts_mov_b64 exec, 1\n\tds_add_u32 %1, %2\n\ts_mov_b64 exec, %0" : "=&s"(keep) : "v"(addr), "v"(1u) : "memory");
}

__global__ void __launch_bounds__(NTHR, 2) fused_decode(Params) {
  u64 pc0;
  u64x8 ka;
  u64x2 kb;
  asm volatile("s_load_dwordx16 %1, %3, 0x0\n\ts_load_dwordx4 %2, %3, 0x40\n\ts_getpc_b64 %0"
               : "=&s"(pc0), "=&s"(ka), "=&s"(kb) : "s"(__builtin_amdgcn_kernarg_segment_ptr()));
  extern __shared__ __attribute__((aligned(16))) unsigned char smem[];
  LAS unsigned char* L = (LAS unsigned char*)smem;
  volatile LAS unsigned* ctl = (volatile LAS unsigned*)(L + L_CNT);
  const unsigned ctl_addr = (unsigned)(size_t)(L + L_CNT);
  const int g = blockIdx.x, tid = threadIdx.x, lane = tid & 63, w = __builtin_amdgcn_readfirstlane(tid >> 6);
  const int grp = g >> 4, mem = g & 15;
  const int kvh = grp & 7, bat = grp >> 3;
  const int jb = (mem + grp) & 3, ji = mem >> 2;
  const bool hasq = jb == 0 || jb == 3;
  const int qt = ((bat * 2 + (jb == 3)) * 2 + (ji & 1)) * 2 + (ji >> 1);
  const bool ffn = jb == 2 && !(bat == 0 && ji == 3);
  const int fj = ((3 + bat) * (grp & 3) + ji + 16 - 16 * bat) * 2 + ((grp >> 2) & 1);
#define GU_PIECE(i) DMA16((const unsigned char*)((lane & 4) ? p.wu : p.wg) + (size_t)((w & 3) * 128 + (i) * 8 + (lane >> 3)) * (FF * 2) + fj * 64 + ((lane & 3) ^ ((lane >> 4) & 1) ^ (((i) & 1) << 1)) * 16, L + L_SLAB + ((w & 3) * 16 + (i)) * 1024)
  const float LOG2E = 1.4426950408889634f;
  {
    unsigned keep;
    const int o0 = lane * 128, o1 = o0 + 8192, o2 = min(o0 + 16384, CODE_BYTES - 128);
    asm volatile("s_cmp_lg_u32 %6, %7\n\ts_cbranch_scc1 1f\n\ts_mov_b32 %0, m0\n\ts_mov_b32 m0, %1\n\ts_nop 0\n\t"
                 "global_load_lds_dword %2, %5\n\tglobal_load_lds_dword %3, %5\n\tglobal_load_lds_dword %4, %5\n\ts_mov_b32 m0, %0\n1:"
                 : "=&s"(keep) : "s"((unsigned)(size_t)(L + L_SINK)), "v"(o0), "v"(o1), "v"(o2), "s"(pc0 & ~(u64)255), "s"(w * 16 + mem), "s"(80 + kvh) : "memory", "scc");
  }
  asm volatile("s_waitcnt lgkmcnt(0)" : "+s"(ka), "+s"(kb) : "s"(qt), "s"(fj), "s"(jb));
  const Params p = {(const u16*)ka[0], (const u16*)ka[1], (const u16*)ka[2], (unsigned char*)ka[3], (const u16*)ka[4], (const u16*)ka[5], (const u16*)ka[6], (const u16*)ka[7], (const u16*)kb[0], (u16*)kb[1]};
  if (w == 4) {
    unsigned keep;
    asm volatile("s_mov_b32 %0, m0\n\ts_mov_b32 m0, %1\n\ts_nop 0\n\tglobal_load_lds_dwordx4 %2, %3\n\ts_add_u32 m0, m0, 0x400\n\ts_nop 0\n\tglobal_load_lds_dwordx4 %4, %3\n\t"
                 "s_add_u32 m0, m0, 0x400\n\ts_nop 0\n\tglobal_load_lds_dword %5, %6 sc1\n\ts_mov_b32 m0, %0"
                 : "=&s"(keep) : "s"((unsigned)(size_t)(L + L_MRG)), "v"(lane * 16), "s"(p.x), "v"(lane * 16 + 1024), "v"(0), "s"(p.ws) : "memory", "scc");
  }

  if (__builtin_expect(w >= 4, 0)) {
    __builtin_amdgcn_s_setprio(3);
    const u64 t_start = NOW();
    const int u = w - 4;
    const int sub = (1 - bat) * 4 + ji;
    const int job = jb == 2 && !ffn ? 4 : jb;
    const int rho = sub * 8 + kvh;
    LAS unsigned char* slab = L + L_SLAB;
    if (u == 0) { if (lane < 16) ctl[lane] = 0u; WAIT_LDS(); }
    __builtin_amdgcn_s_barrier();
    if ((hasq && u > 1) || (job == 4 && u != 0)) { WAIT_VM(); EXIT(); }
    const __amdgpu_buffer_rsrc_t rsw = __builtin_amdgcn_make_buffer_rsrc((void*)p.ws, 0, WS_SPAN, RSRC3);
    u32x4 bf[16], bd[8];
    {
      if (job == 1) {
        const unsigned char* src = (const unsigned char*)p.wo + (size_t)(kvh * 512 + u * 128 + 4 * (lane >> 3)) * 1024 + sub * 128 + (lane & 7) * 16;
#pragma unroll
        for (int i = 0; i < 16; ++i) DMA16(src + (32 * (i >> 2) + (i & 3)) * 1024, slab + (u * 16 + i) * 1024);
      } else if (job == 2) {
#pragma unroll
        for (int i = 0; i < NE; ++i) GU_PIECE(i);
      }
    }
    LAS unsigned char* aread = L + L_A + (lane & 1) * ASTR + ((lane >> 1) & 7) * 64 + (lane >> 4) * 16;
    unsigned tag = 0;
    if (u == 0) {
      WAIT_VM();
      const unsigned ep = *(LAS unsigned*)(L + L_MRG + 2048);
      if (hasq) {
#pragma unroll
        for (int b = 0; b < 2; ++b) {
          const u32x4 xv = *(LAS u32x4*)(L + L_MRG + b * 1024 + lane * 16);
          float v[8];
#pragma unroll
          for (int i = 0; i < 4; ++i) { v[2 * i] = bf_lo(xv[i]); v[2 * i + 1] = bf_hi(xv[i]); }
          float s = 0.f;
#pragma unroll
          for (int i = 0; i < 8; ++i) s += v[i];
          const float mean = wave_sum(s) * (1.0f / DM);
          float q2 = 0.f;
#pragma unroll
          for (int i = 0; i < 8; ++i) q2 = fmaf(v[i] - mean, v[i] - mean, q2);
          const float var = wave_sum(q2) * (1.0f / DM);
          const float mb = rbf(mean), rd = __builtin_amdgcn_rcpf(rbf(__builtin_amdgcn_sqrtf(rbf(rbf(var) + 1e-5f))));
          u32x4 o;
#pragma unroll
          for (int i = 0; i < 4; ++i) o[i] = pk_bf(rbf(v[2 * i] - mb) * rd, rbf(v[2 * i + 1] - mb) * rd);
          *(LAS u32x4*)(L + L_A + b * ASTR + lane * 16) = o;
        }
      }
      tag = __builtin_amdgcn_readfirstlane(ep) + 1u;
      ctl[C_TAG] = tag;
      if (hasq) lds_count(ctl_addr + C_HS * 4);
    }
    if (hasq) {
      lds_wait_ge(ctl + C_HS, 5u);
      tag = __builtin_amdgcn_readfirstlane(ctl[C_TAG]);
      LAS unsigned char* fq = slab + ((lane >> 4) * 8 + ((lane >> 2) & 3)) * 64 + (((lane >> 4) & 1) ^ u) * 32 + (lane & 3) * 8;
#pragma unroll
      for (int s = 0; s < 16; ++s) bf[s] = tr_frag(fq + s * 2048, 256);
      const float v = gemv16(*(LAS u32x4*)aread, *(LAS u32x4*)(aread + 512), lane, bf);
      const float nb = dpp<XOR1>(v);
      if (lane < 32 && !(lane & 1))
        __builtin_amdgcn_raw_buffer_store_b64(u32x2{pk_bf(v, nb), tag}, rsw, WS_Q + ((lane >> 4) * 2048 + kvh * 256 + qt * 16 + u * 8 + ((lane & 15) >> 1)) * 8, 0, AUX_SC1);
      if (u != 0) { WAIT_VM(); EXIT(); }
    }
    if (u == 0) {
      const int qo = WS_Q + (bat * 2048 + kvh * 256 + lane * 2) * 8;
      u32x4 g0, g1;
      wait_until(t_start, QDELAY);
#pragma unroll 1
      for (unsigned sp = 0; sp < SPIN_CAP; ++sp) {
        MEMBAR();
        g0 = __builtin_amdgcn_raw_buffer_load_b128(rsw, qo, 0, AUX_SC1);
        g1 = __builtin_amdgcn_raw_buffer_load_b128(rsw, qo + 1024, 0, AUX_SC1);
        if (__all(g0[1] == tag && g0[3] == tag && g1[1] == tag && g1[3] == tag)) break;
        __builtin_amdgcn_s_sleep(8);
      }
      *(LAS u32x2*)(L + L_Q + lane * 8) = u32x2{g0[0], g0[2]};
      *(LAS u32x2*)(L + L_Q + 512 + lane * 8) = u32x2{g1[0], g1[2]};
      MEMBAR();
      ctl[C_QRDY] = 1u;
    }
    u32x2 xr = {0u, 0u};
    unsigned xp = 0, xq = 0;
    const int ocol = (rho >> 3) * 64 + ((((lane >> 4) & 1) << 1) | (lane >> 5)) * 16 + (rho & 3) * 4 + (lane & 3);
    if (job == 2) {
      xr = *(const GAS u32x2*)((const GAS unsigned char*)p.x + ((lane >> 2) & 1) * 1024 + (u * 128 + (lane >> 3) * 16 + (lane & 3) * 4) * 2);
      xp = *(const GAS u16*)((const GAS unsigned char*)p.x + ((lane >> 2) & 1) * 1024);
      xq = *(const GAS u16*)((const GAS unsigned char*)p.x + ((lane >> 4) & 1) * 1024);
      WAIT_VM();
      lds_count(ctl_addr + C_HS * 4);
    } else if (job == 3) {
      xr[0] = *(const GAS u16*)((const GAS unsigned char*)p.x + ((rho >> 2) & 1) * 1024 + ocol * 2);
    }
    if (job == 1) {
      WAIT_VM();
      lds_count(ctl_addr + C_HS * 4);
      lds_wait_ge(ctl + C_HS, 4u);
      tag = __builtin_amdgcn_readfirstlane(ctl[C_TAG]);
      LAS unsigned char* fa = slab + ((lane >> 4) * 8 + ((lane >> 2) & 3)) * 128 + u * 32 + (lane & 3) * 8;
#pragma unroll
      for (int s = 0; s < 16; ++s) bf[s] = tr_frag(fa + s * 4096, 512);
    }
    if (u == 0) {
      lds_wait_ge(ctl + C_KD, 4u);
      const u64 t_k = NOW();
      const int early = job == 2 ? FE + (((fj >> 1) & 3) == 3 ? FES : 0) : 0;
      const unsigned fin = (unsigned)t_k + NT * TAU / 16 + FB;
      const u32x4 ones = {~0u, ~0u, ~0u, ~0u};
      __builtin_amdgcn_raw_buffer_store_b128(ones, rsw, WS_REC + (kvh * 32 + bat * 16 + mem) * 1024 + lane * 16, 0, AUX_SC1);
      __builtin_amdgcn_s_setprio(0);
      const LAS float* smx = (const LAS float*)(L + L_ST) + (lane & 3);
      const float M = fmaxf(fmaxf(smx[0], smx[4]), fmaxf(smx[8], smx[12]));
      const LAS unsigned char* sp0 = L + L_SC + (lane >> 4) * (NT * 128) + (lane & 15) * 8;
      float ls = 0.f;
#pragma unroll 2
      for (int t = 0; t < NT; ++t) {
        const u32x2 sv = *(const LAS u32x2*)(sp0 + t * 128);
        ls += (__builtin_amdgcn_exp2f(fmaf(bf_lo(sv[0]), LOG2E, -M)) + __builtin_amdgcn_exp2f(fmaf(bf_hi(sv[0]), LOG2E, -M)))
            + (__builtin_amdgcn_exp2f(fmaf(bf_lo(sv[1]), LOG2E, -M)) + __builtin_amdgcn_exp2f(fmaf(bf_hi(sv[1]), LOG2E, -M)));
      }
      ls += dpp<ROR4>(ls); ls += dpp<ROR8>(ls); ls = x16_32_sum(ls);
      __builtin_amdgcn_s_setprio(3);
      WAIT_VM();
      if (lane < 4) __builtin_amdgcn_raw_buffer_store_b128(u32x4{__float_as_uint(M + __builtin_amdgcn_logf(ls)), tag, fin, tag}, rsw, WS_HDR + g * 128 + lane * 16, 0, AUX_SC1);
      u32x4 hd;
#pragma unroll 1
      for (unsigned sp = 0; sp < SPIN_CAP; ++sp) {
        __builtin_amdgcn_s_sleep(24);
        MEMBAR();
        hd = __builtin_amdgcn_raw_buffer_load_b128(rsw, WS_HDR + (grp * 16 + (lane >> 2)) * 128 + (lane & 3) * 16, 0, AUX_SC1);
        if (__all(hd[1] == tag && hd[3] == tag)) break;
      }
      const float v = __uint_as_float(hd[0]);
      float mx = fmaxf(v, dpp<ROR4>(v)); mx = fmaxf(mx, dpp<ROR8>(mx)); mx = x16_32_max(mx);
      float e = __builtin_amdgcn_exp2f(v - mx);
      e += dpp<ROR4>(e); e += dpp<ROR8>(e); e = x16_32_sum(e);
      if (lane < 4) *(LAS float*)(L + L_ST + 64 + lane * 4) = mx + __builtin_amdgcn_logf(e);
      ctl[C_END] = (fin + (unsigned)(int)(wave_sum((float)(int)(hd[2] - fin)) * (1.0f / 64)) - early) | 1u;
      MEMBAR();
      ctl[C_LSE] = 1u;
      if (job == 0 || job == 4) EXIT();
      if (job == 1) {
#pragma unroll 1
        for (unsigned sp = 0; sp < SPIN_CAP; ++sp) {
          MEMBAR();
          const u32x4 t = __builtin_amdgcn_raw_buffer_load_b128(rsw, WS_HDR + ((grp ^ 8) * 16 + (lane & 15)) * 128, 0, AUX_SC1);
          if (__all(t[1] == tag && t[3] == tag)) break;
          __builtin_amdgcn_s_sleep(32);
        }
      }
      if (job != 2) lds_wait_ge(ctl + C_PUB, 1u);
      if (job == 3) {
#pragma unroll 1
        for (unsigned sp = 0; sp < SPIN_CAP; ++sp) {
          MEMBAR();
          const unsigned t = __builtin_amdgcn_raw_buffer_load_b32(rsw, WS_REC + (kvh * 32 + bat * 16 + (lane & 15)) * 1024, 0, AUX_SC1);
          if (__all(t != ~0u)) break;
          __builtin_amdgcn_s_sleep(6);
        }
      }
      ctl[C_GO] = 1u;
    }
    if (job == 2 && u != 0) { lds_wait_ge(ctl + C_QRDY, 1u); tag = __builtin_amdgcn_readfirstlane(ctl[C_TAG]); }
    const unsigned tb = tag & 0xffu;
    unsigned him = 0xffffff00u;
    PIN1(him);
    if (job == 1) {
      int ro = WS_REC + kvh * 32768 + (lane >> 5) * 16384 + ((lane >> 4) & 1) * 1024 + u * 256 + (lane & 15) * 16;
      int ho = WS_HP + (kvh * 32 + sub * 4 + u) * 128 + (lane & 31) * 4;
      unsigned aw = (unsigned)(size_t)(L + L_A) + (lane >> 5) * ASTR + u * 256 + (lane & 15) * 16 + ((lane >> 4) & 1) * 8;
      const unsigned one = ((lane >> 3) & 1) == (lane >> 5) ? 0x3f80u << (16 * (lane & 1)) : 0u;
      u32x4 sel = {(lane & 6) == 0 ? one : 0u, (lane & 6) == 2 ? one : 0u, (lane & 6) == 4 ? one : 0u, (lane & 6) == 6 ? one : 0u};
      PIN4(ro, ho, aw, sel);
      if (u != 0) lds_wait_ge(ctl + C_GO, 1u);
      f32x4 s0, s1;
#pragma unroll 1
      for (unsigned sp = 0; sp < SPIN_CAP; ++sp) {
        MEMBAR();
        u32x4 gr[8];
#pragma unroll
        for (int q = 0; q < 8; ++q) gr[q] = __builtin_amdgcn_raw_buffer_load_b128(rsw, ro, q * 2048, AUX_SC1);
        __builtin_amdgcn_sched_barrier(0);
        unsigned mx = 0;
        s0 = ZERO4; s1 = ZERO4;
#pragma unroll
        for (int q = 0; q < 8; q += 2) {
          s0 = mfma32(sel, gr[q], s0); s1 = mfma32(sel, gr[q + 1], s1);
          mx = max(mx, max(max4(gr[q]), max4(gr[q + 1])));
        }
        if (__all(mx != ~0u)) break;
      }
      s0 += s1;
      *(LAS u32x2*)(size_t)aw = u32x2{pk_bf(s0[0], s0[1]), pk_bf(s0[2], s0[3])};
      BARRIER();
      const float v = gemv16(*(LAS u32x4*)aread, *(LAS u32x4*)(aread + 512), lane, bf);
      __builtin_amdgcn_raw_buffer_store_b32(tagw(v, him, tb), rsw, ho, 0, AUX_SC1);
    } else if (job == 2) {
      int po = WS_HP + u * 1024 + lane * 16;
      int dpo = WS_DP + fj * 4096 + u * 1024 + lane * 16;
      unsigned hw = (unsigned)(size_t)(L + L_A) + ((lane >> 2) & 1) * ASTR + (u * 128 + (lane >> 3) * 16 + (lane & 3) * 4) * 2;
      unsigned sw = (unsigned)(size_t)(L + L_RED) + ((lane >> 2) & 1) * 32 + (lane >> 5) * 16 + u * 4;
      unsigned sr = (unsigned)(size_t)(L + L_RED) + ((lane >> 4) & 1) * 32;
      float pv = bf_lo(xp), pq = bf_lo(xq);
      unsigned m32 = (lane & 32) ? ~0u : 0u;
      unsigned cw = (lane < 32 && (lane & 9) == 0) ? (unsigned)(size_t)(L + L_ACT) + (lane >> 4) * 64 + u * 16 + (lane & 7) * 2 : (unsigned)(size_t)(L + L_SINK) + lane * 4;
      unsigned cr = (unsigned)(size_t)(L + L_ACT) + ((lane >> 2) & 1) * 64 + (lane >> 4) * 16;
      f32x4 xf = {bf_lo(xr[0]), bf_hi(xr[0]), bf_lo(xr[1]), bf_hi(xr[1])};
      unsigned fd = (unsigned)(size_t)(slab + 65536) + ((lane >> 4) * 8 + ((lane >> 2) & 3)) * 1024 + u * 256 + (((lane >> 2) & 3) | (((lane >> 4) & 1) << 2)) * 32 + (lane & 3) * 8;
      PIN4(po, dpo, hw, sw); PIN4(sr, cw, cr, xf); PIN4(fd, pv, pq, m32);
      LAS unsigned char* fa = slab + ((lane >> 4) * 8 + ((lane >> 2) & 3)) * 128 + (lane & 2) * 32 + (u ^ ((lane >> 3) & 3)) * 16 + (lane & 1) * 8;
      lds_wait_ge(ctl + C_HS, 8u);
      u32x4 gp[8];
#pragma unroll
      for (int q = 0; q < 8; ++q) gp[q] = __builtin_amdgcn_raw_buffer_load_b128(rsw, po, q * 4096, AUX_SC1);
      __builtin_amdgcn_sched_barrier(0);
#pragma unroll
      for (int s = 0; s < 16; ++s) bf[s] = tr_frag(fa + s * 4096, 512);
      f32x4 c0 = ZERO4, c1 = ZERO4;
#pragma unroll
      for (int s = 0; s < 16; s += 2) {
        c0 = mfma32(u32x4{0x3f803f80u, 0x3f803f80u, 0x3f803f80u, 0x3f803f80u}, bf[s], c0);
        c1 = mfma32(u32x4{0x3f803f80u, 0x3f803f80u, 0x3f803f80u, 0x3f803f80u}, bf[s + 1], c1);
      }
      __builtin_amdgcn_sched_barrier(0);
#pragma unroll 1
      for (unsigned sp = 0; sp < SPIN_CAP; ++sp) {
        unsigned bad = 0;
#pragma unroll
        for (int q = 0; q < 8; ++q) { gp[q] ^= tb; bad |= or4(gp[q]); }
        if (__all((bad & 0xffu) == 0u)) break;
        MEMBAR();
#pragma unroll
        for (int q = 0; q < 8; ++q) gp[q] = __builtin_amdgcn_raw_buffer_load_b128(rsw, po, q * 4096, AUX_SC1);
        __builtin_amdgcn_sched_barrier(0);
      }
      const float cs = c0[0] + c1[0];
      {
        const f32x4 at = ((F4(gp[0]) + F4(gp[1])) + (F4(gp[2]) + F4(gp[3]))) + ((F4(gp[4]) + F4(gp[5])) + (F4(gp[6]) + F4(gp[7])));
        const unsigned a01 = pk_bf(at[0], at[1]), a23 = pk_bf(at[2], at[3]);
        const unsigned h01 = pk_bf(xf[0] + bf_lo(a01), xf[1] + bf_hi(a01)), h23 = pk_bf(xf[2] + bf_lo(a23), xf[3] + bf_hi(a23));
        *(LAS u32x2*)(size_t)hw = u32x2{h01, h23};
        const float d0 = bf_lo(h01) - pv, d1 = bf_hi(h01) - pv, d2 = bf_lo(h23) - pv, d3 = bf_hi(h23) - pv;
        float t = add32((d0 + d1) + (d2 + d3), fmaf(d0, d0, d1 * d1) + fmaf(d2, d2, d3 * d3));
        t = add16(t, t);
        t += dpp<XOR1>(t); t += dpp<XOR2>(t); t += dpp<ROR8>(t);
        *(LAS float*)(size_t)sw = t;
      }
      BARRIER();
      const u32x4 a0 = *(LAS u32x4*)aread, a1 = *(LAS u32x4*)(aread + 512);
      const f32x4 st0 = *(LAS f32x4*)(size_t)sr, st1 = *(LAS f32x4*)(size_t)(sr + 16);
      __builtin_amdgcn_sched_barrier(0);
#pragma unroll
      for (int t = 0; t < 8; ++t) bd[t] = tr_frag((LAS unsigned char*)(size_t)(fd ^ (t * 32)), 4096);
      __builtin_amdgcn_sched_barrier(0);
      f32x4 dd[8];
      gemv_mm(a0, a1, bf, dd);
      float md = st0[0] + st0[1], sq = st1[0] + st1[1];
      PIN1(md); md += st0[2]; PIN1(sq); sq += st1[2]; PIN1(md); md += st0[3]; PIN1(sq); sq += st1[3];
      md *= 1.0f / DM;
      const float var = fmaf(-md, md, sq * (1.0f / DM));
      float mc = -rbf(pq + md) * cs, rd = __builtin_amdgcn_rcpf(rbf(__builtin_amdgcn_sqrtf(rbf(rbf(fmaxf(var, 0.f)) + 1e-5f))));
#pragma unroll
      for (int k = 0; k < 16; ++k) { __builtin_amdgcn_sched_group_barrier(0x8, 1, 0); __builtin_amdgcn_sched_group_barrier(0x402, 2, 0); }
      asm volatile("" : "+v"(mc), "+v"(rd));
      __builtin_amdgcn_sched_barrier(0);
      const float gv = rbf((gemv_red(dd, lane) + mc) * rd);
      const float uv = dpp<ROR8>(gv);
      const float sg = rbf(gv * __builtin_amdgcn_rcpf(1.0f + __builtin_amdgcn_exp2f(-gv * LOG2E)));
      const float av = sg * uv;
      *(LAS unsigned*)(size_t)cw = pk_bf(av, dpp<XOR1>(av));
      BARRIER();
      const u32x4 aa = *(LAS u32x4*)(size_t)cr;
      u32x4 o;
#pragma unroll
      for (int i = 0; i < 4; ++i) {
        const float e0 = mfma32(aa, bd[i], ZERO4)[0], e1 = mfma32(aa, bd[4 + i], ZERO4)[0];
        o[i] = tagw(__uint_as_float((__float_as_uint(e1) & m32) | (__float_as_uint(e0) & ~m32)), him, tb);
      }
      __builtin_amdgcn_raw_buffer_store_b128(o, rsw, dpo, 0, AUX_SC1);
    } else {
      const int hpo = WS_HP + (ocol >> 4) * 128 + ((rho >> 2) & 1) * 64 + (ocol & 15) * 4;
      wait_until(NOW(), RD);
      unsigned gp[8];
#pragma unroll 1
      for (unsigned sp = 0; sp < SPIN_CAP; ++sp) {
        MEMBAR();
        unsigned bad = 0;
#pragma unroll
        for (int s = 0; s < 8; ++s) { gp[s] = __builtin_amdgcn_raw_buffer_load_b32(rsw, hpo, s * 4096, AUX_SC1) ^ tb; bad |= gp[s]; }
        if (__all((bad & 0xffu) == 0u)) break;
        __builtin_amdgcn_s_sleep(4);
      }
#define U2F(x) __uint_as_float(x)
      float hres = rbf(bf_lo(xr[0]) + rbf(((U2F(gp[0]) + U2F(gp[1])) + (U2F(gp[2]) + U2F(gp[3]))) + ((U2F(gp[4]) + U2F(gp[5])) + (U2F(gp[6]) + U2F(gp[7])))));
      int ao = WS_DP + (lane >> 2) * 4096 + rho * 64 + (lane & 3) * 16;
      int ao3 = lane < 32 ? ao + 3 * 65536 : WS_SPAN;
      int oo = (((rho >> 2) & 1) * 512 + ocol) * 2;
      const __amdgpu_buffer_rsrc_t rso = __builtin_amdgcn_make_buffer_rsrc((void*)p.out, 0, 2048, RSRC3);
      unsigned tb3 = lane < 32 ? tb : 0u;
      PIN4(hres, ao, ao3, oo); PIN1(tb3);
      u32x4 ga[4];
#pragma unroll 1
      for (unsigned sp = 0; sp < SPIN_CAP; ++sp) {
        MEMBAR();
#pragma unroll
        for (int i = 0; i < 3; ++i) ga[i] = __builtin_amdgcn_raw_buffer_load_b128(rsw, ao, i * 65536, AUX_SC1) ^ tb;
        ga[3] = __builtin_amdgcn_raw_buffer_load_b128(rsw, ao3, 0, AUX_SC1) ^ tb3;
        if (__all((or4((ga[0] | ga[1]) | (ga[2] | ga[3])) & 0xffu) == 0u)) break;
      }
      const f32x4 y4 = (F4(ga[0]) + F4(ga[1])) + (F4(ga[2]) + F4(ga[3]));
      float y = add16(add32(y4[0], y4[1]), add32(y4[2], y4[3]));
      y += dpp<ROR8>(y); y += dpp<ROR4>(y);
      __builtin_amdgcn_raw_buffer_store_b16((u16)pk_bf(hres + rbf(y), 0.f), rso, oo, 0, AUX_SC1);
      if (rho == 0 && lane == 0) __builtin_amdgcn_raw_buffer_store_b32(tag, rsw, WS_EPOCH, 0, AUX_SC1);
    }
    EXIT();
  }

  const unsigned tke = (unsigned)NOW() + KEND;
  const int r = lane >> 4, c = lane & 15;
  const size_t base = (size_t)grp * SK * 256;
  u32x4 rs = make_srd((const unsigned char*)p.kc + base, SK * 256);
  const int off0 = grp * 262144 + mem * 16384 + w * 4096;
  const int klane = (c >> 2) * 256 + r * 64 + (c & 3) * 16;
  int off = off0 + klane;
  u32x4 ring[2][4], xk[XW][4];
  if (hasq) {
    const u32x4 rsq = make_srd(p.wq, 512 * 8192);
    const int qo = (lane >> 2) * 8192 + (((lane & 3) ^ ((lane >> 4) & 2)) << 4);
    int so = w * 128 * 8192 + (kvh * 512 + qt * 32) * 2;
    unsigned dst = (unsigned)(size_t)(L + L_SLAB) + w * 8192;
#pragma unroll 1
    for (int i = 0; i < 8; ++i) {
      unsigned keep;
      asm volatile("s_mov_b32 %0, m0\n\ts_mov_b32 m0, %1\n\ts_nop 0\n\tbuffer_load_dwordx4 %2, %3, %4 offen nt lds\n\ts_mov_b32 m0, %0"
                   : "=&s"(keep) : "s"(dst), "v"(qo), "s"(rsq), "s"(so) : "memory");
      so += 16 * 8192; dst += 1024;
    }
  }
  __builtin_amdgcn_sched_barrier(0);
#define STEP off = (off + 262144) & (SK * 256 - 1);
#define FIRST(j, i) LD1I(xk[j][i], off, rs, i);
#define AHEAD1(j, i) WAITN(PACE); LD1I(xk[j][i], off, rs, i);
#define AHEAD(j) if (j > 0) { R4(AHEAD1, j) STEP }
#define RING1(j, i) WAITN(PACE); LD1I(ring[j][i], off, rs, i);
  R4(FIRST, 0)
  STEP
  __builtin_amdgcn_s_barrier();
  if (hasq) { asm volatile("s_waitcnt vmcnt(4)" ::: "memory"); lds_count(ctl_addr + C_HS * 4); }
  R10(AHEAD)
  R4(RING1, 0)
  STEP
  R4(RING1, 1)
  STEP
  __builtin_amdgcn_sched_barrier(0);
  lds_wait_ge(ctl + C_QRDY, 1u);
  const u32x4 qf = *(LAS u32x4*)(L + L_Q + klane);
  WAITN(8);
#define PINX(x) PIN4(xk[x][0], xk[x][1], xk[x][2], xk[x][3]);
  R10(PINX)
  const float SCALE = 0.08838834764831845f;
  LAS unsigned char* sc = L + L_SC + w * (NT * 128) + lane * 2;
  float mL = -1.0e38f;
  auto scores = [&](const f32x4 (&d)[4], int t) {
    float S[4];
#pragma unroll
    for (int i = 0; i < 4; ++i) S[i] = dpp<0xFF>(d[i][3]) + (dpp<0xAA>(d[i][2]) + (dpp<0x55>(d[i][1]) + dpp<0x00>(d[i][0])));
    const float lo = (c & 1) ? S[1] : S[0], hi = (c & 1) ? S[3] : S[2];
    const float x = rbf((c & 2) ? hi : lo) * SCALE;
    const unsigned b = pk_bf(x, x);
    mL = fmaxf(mL, bf_lo(b));
    *(LAS u16*)(sc + t * 128) = (u16)b;
  };
#pragma unroll 1
  for (int step = XW; step < NT; step += 2) {
    if (step + 2 == NT) {
      rs = make_srd((const unsigned char*)p.vc + base, SK * 256);
      off = off0 + lane * 16;
    }
    const bool ahead = (int)(tke - (unsigned)NOW()) * 16 > (NT - step) * TAUK;
    f32x4 d[4];
#define KLD(j, i) if (ahead) WAITN(KLO); WAITD(ring[j][i], PHI); d[i] = mfma32(ring[j][i], qf, ZERO4); LD1(ring[j][i], off, rs, i); __builtin_amdgcn_sched_barrier(0);
#define XMM(x, i) e[i] = mfma32(xk[x][i], qf, ZERO4);
#define XTILE(j, y) if (step == XW + 2 * y) { R4(XMM, 2 * y + j) }
#define XT(j) if (step < 2 * XW) { f32x4 e[4] = {ZERO4, ZERO4, ZERO4, ZERO4}; XTILE(j, 0) XTILE(j, 1) XTILE(j, 2) XTILE(j, 3) XTILE(j, 4) scores(e, step - XW + j); }
#define KSTEP(j) KLD(j, 0) KLD(j, 1) XT(j) KLD(j, 2) KLD(j, 3) STEP scores(d, step + j);
    KSTEP(0)
    KSTEP(1)
  }
  mL = fmaxf(mL, dpp<XOR1>(mL)); mL = fmaxf(mL, dpp<XOR2>(mL));
  mL = x16_32_max(mL) * LOG2E;
  *(LAS float*)(L + L_ST + w * 16 + (c >> 2) * 4) = mL;
  lds_count(ctl_addr + C_KD * 4);
  const unsigned mask0 = (c & 3) == 0 ? 0x0000ffffu : ((c & 3) == 1 ? 0xffff0000u : 0u);
  const unsigned mask1 = (c & 3) == 2 ? 0x0000ffffu : ((c & 3) == 3 ? 0xffff0000u : 0u);
  f32x4 o1 = ZERO4, o2 = ZERO4;
  const unsigned tv0 = (tke + (int)((unsigned)NOW() - tke + 30) / 2 + NT * TAU / 16 - (ffn ? FE : 0)) | 1u;
  u32x2 bp[4];
  float f = 0.f;
  auto probs = [&](int t) {
    const float pr = __builtin_amdgcn_exp2f(fmaf(bf_lo(*(LAS u16*)(sc + t * 128)), LOG2E, -mL));
    const unsigned b = pk_bf(pr, pr);
    bp[0] = u32x2{dppu<0x00>(b) & mask0, dppu<0x00>(b) & mask1}; bp[1] = u32x2{dppu<0x55>(b) & mask0, dppu<0x55>(b) & mask1};
    bp[2] = u32x2{dppu<0xAA>(b) & mask0, dppu<0xAA>(b) & mask1}; bp[3] = u32x2{dppu<0xFF>(b) & mask0, dppu<0xFF>(b) & mask1};
    PIN4(bp[0], bp[1], bp[2], bp[3]);
  };
#define VLD(j, i) if (ahead) WAITN(PLO); WAITD(ring[j][i], PHI); \
    o1 = mfma16(u32x2{ring[j][i][0], ring[j][i][1]}, bp[i], o1); o2 = mfma16(u32x2{ring[j][i][2], ring[j][i][3]}, bp[i], o2); LD1(ring[j][i], off, rs, i);
#pragma unroll 1
  for (int t = 0; t < NT; t += 2) {
    unsigned te = __builtin_amdgcn_readfirstlane(ctl[C_END]);
    if (!te) te = tv0;
    const bool ahead = (int)(te - (unsigned)NOW()) * 16 > (NT - t) * TAU;
    if (t + 2 == NT) {
      rs[2] = 0;
      lds_wait_ge(ctl + C_LSE, 1u);
      f = __builtin_amdgcn_exp2f(mL - *(LAS float*)(L + L_ST + 64 + (c >> 2) * 4));
    }
    probs(t);
    R4(VLD, 0)
    STEP
    probs(t + 1);
    R4(VLD, 1)
    STEP
  }
  LAS f32x4* mg = (LAS f32x4*)(L + L_MRG) + lane;
  mg[w * 128] = f32x4{o1[0] * f, o2[0] * f, o1[1] * f, o2[1] * f};
  mg[w * 128 + 64] = f32x4{o1[2] * f, o2[2] * f, o1[3] * f, o2[3] * f};
  unsigned tk; u64 keep;
  f32x4 m0, m1, m2, m3, m4, m5, m6, m7;
  asm volatile("s_mov_b64 %1, exec\n\ts_mov_b64 exec, 1\n\tds_add_rtn_u32 %0, %10, %11\n\ts_mov_b64 exec, %1\n\t"
               "ds_read_b128 %2, %12\n\tds_read_b128 %3, %12 offset:2048\n\tds_read_b128 %4, %12 offset:4096\n\tds_read_b128 %5, %12 offset:6144\n\t"
               "ds_read_b128 %6, %12 offset:1024\n\tds_read_b128 %7, %12 offset:3072\n\tds_read_b128 %8, %12 offset:5120\n\tds_read_b128 %9, %12 offset:7168\n\ts_waitcnt lgkmcnt(0)"
               : "=&v"(tk), "=&s"(keep), "=&v"(m0), "=&v"(m1), "=&v"(m2), "=&v"(m3), "=&v"(m4), "=&v"(m5), "=&v"(m6), "=&v"(m7)
               : "v"(ctl_addr + C_MRG * 4), "v"(1u), "v"((unsigned)(size_t)mg) : "memory");
  if (__builtin_amdgcn_readfirstlane(tk) == 3) {
    const __amdgpu_buffer_rsrc_t rsw = __builtin_amdgcn_make_buffer_rsrc((void*)p.ws, 0, WS_SPAN, RSRC3);
    const int ro = WS_REC + (kvh * 32 + bat * 16 + mem) * 1024 + (c >> 2) * 256 + (r * 4 + (c & 3)) * 16;
    const f32x4 a = (m0 + m1) + (m2 + m3), b = (m4 + m5) + (m6 + m7);
    __builtin_amdgcn_raw_buffer_store_b128(u32x4{min(pk_bf(a[0], a[1]), ~1u), min(pk_bf(a[2], a[3]), ~1u), min(pk_bf(b[0], b[1]), ~1u), min(pk_bf(b[2], b[3]), ~1u)}, rsw, ro, 0, AUX_SC1);
    MEMBAR();
    ctl[C_PUB] = 1u;
  }
  if (ffn) {
    lds_wait_ge(ctl + C_PUB, 1u);
#pragma unroll
    for (int i = NE; i < 16; ++i) GU_PIECE(i);
#pragma unroll
    for (int i = 0; i < 8; ++i) DMA16((const unsigned char*)p.wd + (size_t)(fj * 32 + w * 8 + i) * 1024 + (lane ^ (((i & 3) | ((w & 1) << 2)) << 1)) * 16, L + L_SLAB + 65536 + (w * 8 + i) * 1024);
    asm volatile("s_waitcnt vmcnt(8)" ::: "memory");
    lds_count(ctl_addr + C_HS * 4);
  }
  asm volatile("s_waitcnt vmcnt(0)" : "+v"(ring[0][0]), "+v"(ring[0][1]), "+v"(ring[0][2]), "+v"(ring[0][3]), "+v"(ring[1][0]), "+v"(ring[1][1]), "+v"(ring[1][2]), "+v"(ring[1][3]) :: "memory");
  EXIT();
}

extern "C" void kernel_launch(void* const* d_in, const int* in_sizes, int n_in, void* d_out, int out_size, void* d_ws, size_t ws_size, hipStream_t stream) {
  Params p;
  p.x = (const u16*)d_in[0]; p.kc = (const u16*)d_in[1]; p.vc = (const u16*)d_in[2]; p.wq = (const u16*)d_in[3]; p.wo = (const u16*)d_in[4];
  p.wg = (const u16*)d_in[6]; p.wu = (const u16*)d_in[7]; p.wd = (const u16*)d_in[8];
  p.out = (u16*)d_out; p.ws = (unsigned char*)d_ws;
  (void)hipFuncSetAttribute((const void*)fused_decode, hipFuncAttributeMaxDynamicSharedMemorySize, L_BYTES);
  hipLaunchKernelGGL(fused_decode, dim3(NWG), dim3(NTHR), L_BYTES, stream, p);
}
```
